```python
import torch

def _softmax(x, axis=-1):
    return torch.softmax(x, dim=axis)

def _gelu(x):
    return torch.nn.functional.gelu(x, approximate='tanh')

def _silu(x):
    return torch.nn.functional.silu(x)

def _sigmoid(x):
    return torch.sigmoid(x)

def _layernorm(x, axis=-1, eps=1e-05):
    m = x.mean(dim=axis, keepdim=True)
    v = x.var(dim=axis, keepdim=True, unbiased=False)
    return (x - m) / torch.sqrt(v + eps)

def _scatter_add(tgt, idx, src, axis=0):
    return tgt.clone().index_add_(axis, idx, src)

def _segment_sum(src, seg, n):
    out = torch.zeros((n,) + src.shape[1:], dtype=src.dtype)
    return out.index_add_(0, seg, src)

def _pad(x, pw):
    flat = tuple((v for pair in reversed(pw) for v in pair))
    return torch.nn.functional.pad(x, flat)
kernel_name = 'concat_heads__memory_aug_recurrent_mem_tr__sliding_local_dilated__prompt'

def setup_inputs():

    _w = lambda a: (lambda t: t.to(torch.bfloat16) if t.is_floating_point() else t)(torch.as_tensor(a))

    def _A_setup():
        B, S, Dm = (1, 2048, 640)
        H, Dh = (32, 128)
        M, seg = (8, 32)
        seg = min(seg, S)
        S = S // seg * seg
        return {'x': _w((lambda *s: torch.randn(*s, dtype=torch.bfloat16) * 0.05)(B, S, Dm)), 'mem0': _w((lambda *s: torch.randn(*s, dtype=torch.bfloat16) * 1.6)(B, M, Dm)), 'Wq': _w((lambda *s: torch.randn(*s, dtype=torch.bfloat16) * 0.8)(Dm, H, Dh) * Dm ** (-0.5)), 'Wk': _w((lambda *s: torch.randn(*s, dtype=torch.bfloat16) * 0.8)(Dm, H, Dh) * Dm ** (-0.5)), 'Wv': _w((lambda *s: torch.randn(*s, dtype=torch.bfloat16) * 1.6)(Dm, H, Dh) * Dm ** (-0.5)), 'Wo': _w((lambda *s: torch.randn(*s, dtype=torch.bfloat16) * 1.6)(H, Dh, Dm) * (H * Dh) ** (-0.5)), 'seg_len': seg}
    _A_in = _A_setup()

    def _B_setup():
        B, S, D = (1, 2048, 640)
        return {'x': _w((lambda *s: torch.randn(*s, dtype=torch.bfloat16) * 0.2)(B, S, D)), 'wqkv': _w((lambda *s: torch.randn(*s, dtype=torch.bfloat16) * 0.2)(D, 3 * D)), 'wo': _w((lambda *s: torch.randn(*s, dtype=torch.bfloat16) * 0.05)(D, D))}
    _B_in = _B_setup()
    return {'A_x': _A_in['x'], 'A_mem0': _A_in['mem0'], 'A_Wq': _A_in['Wq'], 'A_Wk': _A_in['Wk'], 'A_Wv': _A_in['Wv'], 'A_Wo': _A_in['Wo'], 'A_seg_len': _A_in['seg_len'], 'B_x': _B_in['x'], 'B_wqkv': _B_in['wqkv'], 'B_wo': _B_in['wo']}

def reference(A_x, A_mem0, A_Wq, A_Wk, A_Wv, A_Wo, A_seg_len, B_x, B_wqkv, B_wo):

    def _A_ref(x, mem0, Wq, Wk, Wv, Wo, seg_len):
        B, S, Dm = x.shape
        H, Dh = (Wq.shape[1], Wq.shape[2])
        M = mem0.shape[1]
        scale = Dh ** (-0.5)
        n_seg = S // seg_len
        L = M + seg_len
        idx = torch.arange(L)
        mask = (idx[:, None] < M) | (idx[:, None] >= idx[None, :])
        mem = mem0
        outs = []
        for s in range(n_seg):
            xs = x[:, s * seg_len:(s + 1) * seg_len, :].clone()
            h = torch.concatenate([mem, xs], axis=1)
            q = torch.einsum('btd,dhe->bhte', h, Wq)
            k = torch.einsum('btd,dhe->bhte', h, Wk)
            v = torch.einsum('btd,dhe->bhte', h, Wv)
            scores = torch.einsum('bhqe,bhke->bhqk', q, k) * scale
            scores = torch.where(mask[None, None], scores, -1000000000.0)
            p = _softmax(scores, axis=-1)
            o = torch.einsum('bhqk,bhke->bhqe', p, v)
            o = torch.einsum('bhte,hed->btd', o, Wo)
            mem = o[:, :M, :].clone()
            outs.append(o[:, M:, :])
        return torch.concatenate(outs, axis=1)

    def _B_ref(x, wqkv, wo):
        B, S, D = x.shape
        H = 32
        Dh = D // H
        W, dil = (8, 2)
        qkv = (lambda x, *d: x.permute(*d))((x @ wqkv).reshape(B, S, 3, H, Dh), 2, 0, 3, 1, 4)
        q, k, v = (qkv[0], qkv[1], qkv[2])
        scale = Dh ** (-0.5)
        pos = torch.arange(S)
        off = torch.arange(W) * dil
        idx = pos[:, None] - off[None, :]
        mask = idx >= 0
        idx_c = torch.where(mask, idx, 0).reshape(-1)
        kg = k[:, :, idx_c, :].reshape(B, H, S, W, Dh)
        vg = v[:, :, idx_c, :].reshape(B, H, S, W, Dh)
        logits = torch.einsum('bhsd,bhswd->bhsw', q, kg) * scale
        logits = torch.where(mask[None, None, :, :], logits, -1000000000.0)
        attn = _softmax(logits, axis=-1)
        o = torch.einsum('bhsw,bhswd->bhsd', attn, vg)
        return (lambda x, *d: x.permute(*d))(o, 0, 2, 1, 3).reshape(B, S, D) @ wo
    a = _A_ref(A_x, A_mem0, A_Wq, A_Wk, A_Wv, A_Wo, A_seg_len)
    b = _B_ref(B_x, B_wqkv, B_wo)
    return torch.concatenate([a, b], axis=-1)
```

```cpp
#include <hip/hip_runtime.h>
#include <hip/hip_bf16.h>
#include <stdint.h>

#define GAS __attribute__((address_space(1)))
#define LAS __attribute__((address_space(3)))
typedef unsigned short u16;
typedef short bf16x8 __attribute__((ext_vector_type(8)));
typedef short bf16x4 __attribute__((ext_vector_type(4)));
typedef float f32x4 __attribute__((ext_vector_type(4)));
typedef unsigned v4u __attribute__((ext_vector_type(4)));
typedef unsigned v2u __attribute__((ext_vector_type(2)));
typedef GAS unsigned long long gu64;
typedef GAS unsigned gu32;
typedef LAS bf16x4 lds_bf16x4;
#define RLX_AGENT __ATOMIC_RELAXED, __HIP_MEMORY_SCOPE_AGENT

constexpr int SEQ = 2048, DM = 640, NH = 32, DH = 128, MEMN = 8, SEG = 32, NSEG = 64, HD = 4096;
constexpr int BD3 = 1920, BDH = 20;

constexpr size_t XA_OFF = 0;
constexpr size_t XA_BYTES = (size_t)2 * 64 * 512 * 8;
constexpr size_t XB_OFF = XA_OFF + XA_BYTES;
constexpr size_t XB_BYTES = (size_t)2 * 64 * 2560 * 8;
constexpr size_t XC_OFF = XB_OFF + XB_BYTES;
constexpr size_t XC_BYTES = (size_t)2 * 2560 * 8;
constexpr size_t TMO_OFF = XC_OFF + XC_BYTES;
constexpr size_t XCHG_BYTES = TMO_OFF + 256;
constexpr size_t Q_OFF = (XCHG_BYTES + 4095) / 4096 * 4096;
constexpr size_t K_OFF = Q_OFF + (size_t)SEQ * HD * 2;
constexpr size_t VT_OFF = K_OFF + (size_t)SEQ * HD * 2;
constexpr size_t KM_OFF = VT_OFF + (size_t)SEQ * HD * 2;
constexpr size_t VMT_OFF = KM_OFF + (size_t)NSEG * MEMN * HD * 2;
constexpr size_t O_OFF = VMT_OFF + (size_t)NSEG * MEMN * HD * 2;
constexpr size_t BQKV_OFF = O_OFF + (size_t)SEQ * HD * 2;
constexpr size_t OB_OFF = BQKV_OFF + (size_t)SEQ * BD3 * 2;
constexpr size_t WS_END = OB_OFF + (size_t)SEQ * DM * 2;

__device__ __forceinline__ u16 f2bf(float f) { return __builtin_bit_cast(u16, (__bf16)f); }
__device__ __forceinline__ float bf2f(u16 b) { return __builtin_bit_cast(float, (unsigned)b << 16); }
__device__ __forceinline__ float rbf(float f) { return bf2f(f2bf(f)); }
__device__ __forceinline__ f32x4 mfma32(bf16x8 a, bf16x8 b, f32x4 c) { return __builtin_amdgcn_mfma_f32_16x16x32_bf16(a, b, c, 0, 0, 0); }

__device__ __forceinline__ unsigned off_b(unsigned row, unsigned ch) { return 256u * row + 16u * (ch ^ (((row & 3) << 2) | ((row >> 2) & 3))); }

template <int MODE>
__device__ __forceinline__ void gemm_tile(const u16* __restrict__ A, int lda, const u16* __restrict__ W, int ldw,
                                          u16* __restrict__ C, int ldc, int K, int m0, int n0, unsigned char* lds) {
    const int tid = threadIdx.x, w = tid >> 6, lane = tid & 63, c16 = lane & 15, g = lane >> 4;
    const int wm = w >> 1, wn = w & 1;
    unsigned char* LA = lds;
    unsigned char* LW = lds + 32768;
    f32x4 acc[4][4];
#pragma unroll
    for (int i = 0; i < 4; ++i)
#pragma unroll
        for (int jn = 0; jn < 4; ++jn) acc[i][jn] = (f32x4){0.f, 0.f, 0.f, 0.f};
    v4u ra[4], rw[4];
    auto gload = [&](int kt) {
#pragma unroll
        for (int i = 0; i < 4; ++i) {
            const int c = tid + 256 * i;
            ra[i] = *(const v4u*)(A + (size_t)(m0 + (c >> 3)) * lda + kt * 64 + (c & 7) * 8);
            rw[i] = *(const v4u*)(W + (size_t)(kt * 64 + (c >> 4)) * ldw + n0 + (c & 15) * 8);
        }
    };
    auto lstore = [&](int buf) {
#pragma unroll
        for (int i = 0; i < 4; ++i) {
            const int c = tid + 256 * i;
            const int row = c >> 3, ch = c & 7;
            *(v4u*)(LA + buf * 16384 + row * 128 + 16 * (ch ^ (row & 7))) = ra[i];
            const int r2 = c >> 4, ch2 = c & 15;
            *(v4u*)(LW + buf * 16384 + (r2 >> 5) * 8192 + off_b(r2 & 31, ch2)) = rw[i];
        }
    };
    const int nk = K / 64;
    gload(0); lstore(0); __syncthreads();
    const unsigned q4 = (lane & 15) >> 2, p4 = lane & 3;
    for (int kt = 0; kt < nk; ++kt) {
        const int buf = kt & 1;
        if (kt + 1 < nk) gload(kt + 1);
#pragma unroll
        for (int ks = 0; ks < 2; ++ks) {
            bf16x8 af[4], wf[4];
#pragma unroll
            for (int mt = 0; mt < 4; ++mt) {
                const int row = wm * 64 + mt * 16 + c16, ch = ks * 4 + g;
                af[mt] = *(const bf16x8*)(LA + buf * 16384 + row * 128 + 16 * (ch ^ (row & 7)));
            }
#pragma unroll
            for (int nt = 0; nt < 4; ++nt) {
                const unsigned cb = wn * 4 + nt;
                unsigned char* base = LW + buf * 16384 + ks * 8192;
                const bf16x4 lo = __builtin_amdgcn_ds_read_tr16_b64_v4i16((lds_bf16x4*)(base + off_b(8 * g + q4, 2 * cb + (p4 >> 1)) + 8 * (p4 & 1)));
                const bf16x4 hi = __builtin_amdgcn_ds_read_tr16_b64_v4i16((lds_bf16x4*)(base + off_b(8 * g + 4 + q4, 2 * cb + (p4 >> 1)) + 8 * (p4 & 1)));
                wf[nt] = __builtin_shufflevector(lo, hi, 0, 1, 2, 3, 4, 5, 6, 7);
            }
#pragma unroll
            for (int mt = 0; mt < 4; ++mt)
#pragma unroll
                for (int nt = 0; nt < 4; ++nt)
                    acc[mt][nt] = (MODE == 0) ? mfma32(wf[nt], af[mt], acc[mt][nt]) : mfma32(af[mt], wf[nt], acc[mt][nt]);
        }
        if (kt + 1 < nk) lstore(buf ^ 1);
        __syncthreads();
    }
#pragma unroll
    for (int mt = 0; mt < 4; ++mt)
#pragma unroll
        for (int nt = 0; nt < 4; ++nt) {
            v2u pk;
            pk.x = (unsigned)f2bf(acc[mt][nt][0]) | ((unsigned)f2bf(acc[mt][nt][1]) << 16);
            pk.y = (unsigned)f2bf(acc[mt][nt][2]) | ((unsigned)f2bf(acc[mt][nt][3]) << 16);
            if (MODE == 0) {
                const int m = m0 + wm * 64 + mt * 16 + c16, n = n0 + wn * 64 + nt * 16 + 4 * g;
                *(v2u*)(C + (size_t)m * ldc + n) = pk;
            } else {
                const int m = m0 + wm * 64 + mt * 16 + 4 * g, n = n0 + wn * 64 + nt * 16 + c16;
                *(v2u*)(C + ((size_t)(m >> 5) * HD + n) * 32 + (m & 31)) = pk;
            }
        }
}

struct Gemm1Args { const u16* Ax; const u16* Wq; const u16* Wk; const u16* Wv; const u16* Bx; const u16* Bw; u16* Q; u16* K; u16* Vt; u16* Bqkv; };
__global__ __launch_bounds__(256) void gemm1_kernel(Gemm1Args a) {
    __shared__ __attribute__((aligned(16))) unsigned char lds[65536];
    int b = blockIdx.x;
    if (b < 16 * 96) {
        const int mt = b & 15, nt = b >> 4;
        const int which = nt >> 5, n0 = (nt & 31) * 128, m0 = mt * 128;
        if (which == 0) gemm_tile<0>(a.Ax, DM, a.Wq, HD, a.Q, HD, DM, m0, n0, lds);
        else if (which == 1) gemm_tile<0>(a.Ax, DM, a.Wk, HD, a.K, HD, DM, m0, n0, lds);
        else gemm_tile<1>(a.Ax, DM, a.Wv, HD, a.Vt, HD, DM, m0, n0, lds);
    } else {
        b -= 16 * 96;
        const int mt = b & 15, nt = b >> 4;
        gemm_tile<0>(a.Bx, DM, a.Bw, BD3, a.Bqkv, BD3, DM, mt * 128, nt * 128, lds);
    }
}

struct Gemm2Args { const u16* O; const u16* Wo; const u16* Ob; const u16* Bwo; u16* out; };
__global__ __launch_bounds__(256) void gemm2_kernel(Gemm2Args a) {
    __shared__ __attribute__((aligned(16))) unsigned char lds[65536];
    int b = blockIdx.x;
    if (b < 80) {
        const int mt = b & 15, nt = b >> 4;
        gemm_tile<0>(a.O, HD, a.Wo, DM, a.out, 2 * DM, HD, mt * 128, nt * 128, lds);
    } else {
        b -= 80;
        const int mt = b & 15, nt = b >> 4;
        gemm_tile<0>(a.Ob, DM, a.Bwo, DM, a.out + DM, 2 * DM, DM, mt * 128, nt * 128, lds);
    }
}

struct ChainArgs { const u16* mem0; const u16* Wq; const u16* Wk; const u16* Wv; const u16* Wo; const u16* Kx; const u16* Vt;
                   u16* Km; u16* Vmt; unsigned long long* xa; unsigned long long* xb; unsigned long long* xc; unsigned* tmo; };

constexpr unsigned kSpinCap = 1u << 17;

template <int NPT, int TAGSHIFT>
__device__ __forceinline__ void gather(gu64* src, unsigned tag, unsigned long long (&v)[NPT], gu32* tmo) {
    const int t = threadIdx.x;
#pragma unroll
    for (int k = 0; k < NPT; ++k) v[k] = __hip_atomic_load(src + t + 256 * k, RLX_AGENT);
    for (unsigned spins = 0;; ++spins) {
        bool ok = true;
#pragma unroll
        for (int k = 0; k < NPT; ++k) ok &= ((unsigned)(v[k] >> TAGSHIFT) == tag);
        if (__all(ok)) break;
        if (spins > kSpinCap) { __hip_atomic_store(tmo, 1u, RLX_AGENT); break; }
        if ((spins & 63u) == 63u && __hip_atomic_load(tmo, RLX_AGENT) != 0u) break;
#pragma unroll
        for (int k = 0; k < NPT; ++k)
            if ((unsigned)(v[k] >> TAGSHIFT) != tag) v[k] = __hip_atomic_load(src + t + 256 * k, RLX_AGENT);
    }
}

constexpr int MEM_STRIDE = 1296, QK_STRIDE = 272;
constexpr int L_MEM = 0, L_Q = L_MEM + 8 * MEM_STRIDE, L_K = L_Q + 8 * QK_STRIDE, L_VT = L_K + 8 * QK_STRIDE, L_RED = L_VT + 128 * 16, L_END = L_RED + 32 * 80 * 4;

__global__ __launch_bounds__(256, 1) void chain_kernel(ChainArgs a) {
    __shared__ __attribute__((aligned(16))) unsigned char lds[51200];
    static_assert(L_END <= 51200, "lds");
    const int cu = blockIdx.x, h = cu >> 1, j = cu & 1;
    const int tid = threadIdx.x, w = tid >> 6, lane = tid & 63, c16 = lane & 15, g = lane >> 4;
    gu32* tmo = (gu32*)a.tmo;

    bf16x8 wqkv[3][20];
    bf16x8 wo[5][4];
    {
        const u16* Wqk = j ? a.Wk : a.Wq;
        const unsigned q4 = (lane & 15) >> 2, p4 = lane & 3;
#pragma unroll
        for (int rd = 0; rd < 5; ++rd) {
            v4u tmp[12];
#pragma unroll
            for (int i = 0; i < 12; ++i) {
                const int c = tid + 256 * i;
                const int row = c / 24, ch = c - row * 24;
                const size_t kr = (size_t)(rd * 128 + row) * HD + h * DH;
                tmp[i] = (ch < 16) ? *(const v4u*)(Wqk + kr + ch * 8) : *(const v4u*)(a.Wv + kr + j * 64 + (ch - 16) * 8);
            }
#pragma unroll
            for (int i = 0; i < 12; ++i) {
                const int c = tid + 256 * i;
                const int row = c / 24, ch = c - row * 24;
                *(v4u*)(lds + row * 400 + ch * 16) = tmp[i];
            }
            __syncthreads();
#pragma unroll
            for (int k4 = 0; k4 < 4; ++k4)
#pragma unroll
                for (int t = 0; t < 3; ++t) {
                    const int col = (t < 2) ? ((2 * w + t) * 16) : (128 + w * 16);
                    unsigned char* p0 = lds + (k4 * 32 + 8 * g + q4) * 400 + (col + 4 * p4) * 2;
                    const bf16x4 lo = __builtin_amdgcn_ds_read_tr16_b64_v4i16((lds_bf16x4*)p0);
                    const bf16x4 hi = __builtin_amdgcn_ds_read_tr16_b64_v4i16((lds_bf16x4*)(p0 + 4 * 400));
                    wqkv[t][rd * 4 + k4] = __builtin_shufflevector(lo, hi, 0, 1, 2, 3, 4, 5, 6, 7);
                }
            __syncthreads();
        }
#pragma unroll
        for (int rd = 0; rd < 2; ++rd) {
            v4u tmp[10];
#pragma unroll
            for (int i = 0; i < 10; ++i) {
                const int c = tid + 256 * i;
                const int row = c / 40, ch = c - row * 40;
                tmp[i] = *(const v4u*)(a.Wo + (size_t)(h * DH + rd * 64 + row) * DM + j * 320 + ch * 8);
            }
#pragma unroll
            for (int i = 0; i < 10; ++i) {
                const int c = tid + 256 * i;
                const int row = c / 40, ch = c - row * 40;
                *(v4u*)(lds + row * 656 + ch * 16) = tmp[i];
            }
            __syncthreads();
#pragma unroll
            for (int k2 = 0; k2 < 2; ++k2)
#pragma unroll
                for (int nt = 0; nt < 5; ++nt) {
                    unsigned char* p0 = lds + (k2 * 32 + 4 * g + q4) * 656 + (w * 80 + nt * 16 + 4 * p4) * 2;
                    const bf16x4 lo = __builtin_amdgcn_ds_read_tr16_b64_v4i16((lds_bf16x4*)p0);
                    const bf16x4 hi = __builtin_amdgcn_ds_read_tr16_b64_v4i16((lds_bf16x4*)(p0 + 16 * 656));
                    wo[nt][rd * 2 + k2] = __builtin_shufflevector(lo, hi, 0, 1, 2, 3, 4, 5, 6, 7);
                }
            __syncthreads();
        }
    }
    const float scale = 0.08838834764831845f;

    for (int s = 0; s < NSEG; ++s) {
        const unsigned tag = (unsigned)(s + 1);
        const int par = s & 1;
        bf16x8 kx[2][4], vx[8];
#pragma unroll
        for (int kt = 0; kt < 2; ++kt)
#pragma unroll
            for (int ks = 0; ks < 4; ++ks)
                kx[kt][ks] = *(const bf16x8*)(a.Kx + (size_t)(s * SEG + kt * 16 + c16) * HD + h * DH + 32 * ks + 8 * g);
#pragma unroll
        for (int et = 0; et < 8; ++et) {
            const u16* p = a.Vt + ((size_t)s * HD + h * DH + et * 16 + c16) * 32;
            const bf16x4 lo = *(const bf16x4*)(p + 4 * g), hi = *(const bf16x4*)(p + 16 + 4 * g);
            vx[et] = __builtin_shufflevector(lo, hi, 0, 1, 2, 3, 4, 5, 6, 7);
        }
        if (s == 0) {
            for (int i = tid; i < 8 * 80; i += 256) {
                const int tk = i / 80, ch = i % 80;
                *(v4u*)(lds + L_MEM + tk * MEM_STRIDE + ch * 16) = *(const v4u*)(a.mem0 + tk * DM + ch * 8);
            }
        } else {
            unsigned long long v[10];
            gather<10, 32>((gu64*)a.xc + (size_t)((s - 1) & 1) * 2560, (unsigned)s, v, tmo);
#pragma unroll
            for (int k = 0; k < 10; ++k)
                *(unsigned*)(lds + L_MEM + ((tid >> 1) & 7) * MEM_STRIDE + ((tid >> 4) + 16 * k) * 8 + (tid & 1) * 4) = (unsigned)v[k];
        }
        __syncthreads();
        f32x4 acc[3];
#pragma unroll
        for (int t = 0; t < 3; ++t) acc[t] = (f32x4){0.f, 0.f, 0.f, 0.f};
#pragma unroll
        for (int ks = 0; ks < 20; ++ks) {
            const bf16x8 b = *(const bf16x8*)(lds + L_MEM + (c16 & 7) * MEM_STRIDE + ks * 64 + g * 16);
#pragma unroll
            for (int t = 0; t < 3; ++t) acc[t] = mfma32(wqkv[t][ks], b, acc[t]);
        }
        u16 qv[3][4];
#pragma unroll
        for (int t = 0; t < 3; ++t)
#pragma unroll
            for (int r = 0; r < 4; ++r) qv[t][r] = f2bf(acc[t][r]);
        if (c16 < 8) {
            const int token = c16;
            if (s + 1 < NSEG) {
                gu64* dst = (gu64*)a.xa + ((size_t)par * 64 + cu) * 512 + (w * 32 + g * 8 + token) * 4;
#pragma unroll
                for (int r = 0; r < 4; ++r)
                    __hip_atomic_store(dst + r, (unsigned long long)qv[0][r] | ((unsigned long long)qv[1][r] << 16) | ((unsigned long long)qv[2][r] << 32) | ((unsigned long long)tag << 48), RLX_AGENT);
            }
            unsigned char* qk = lds + (j ? L_K : L_Q) + token * QK_STRIDE;
#pragma unroll
            for (int t = 0; t < 2; ++t) {
                v2u pk; pk.x = (unsigned)qv[t][0] | ((unsigned)qv[t][1] << 16); pk.y = (unsigned)qv[t][2] | ((unsigned)qv[t][3] << 16);
                *(v2u*)(qk + ((2 * w + t) * 16 + 4 * g) * 2) = pk;
                if (j) *(v2u*)(a.Km + (size_t)(s * MEMN + token) * HD + h * DH + (2 * w + t) * 16 + 4 * g) = pk;
            }
#pragma unroll
            for (int r = 0; r < 4; ++r) {
                const int e = j * 64 + w * 16 + 4 * g + r;
                *(u16*)(lds + L_VT + e * 16 + token * 2) = qv[2][r];
                a.Vmt[((size_t)s * HD + h * DH + e) * 8 + token] = qv[2][r];
            }
        }
        if (s + 1 == NSEG) break;
        {
            unsigned long long v[2];
            gather<2, 48>((gu64*)a.xa + ((size_t)par * 64 + (cu ^ 1)) * 512, tag, v, tmo);
            const int pj = j ^ 1;
#pragma unroll
            for (int k = 0; k < 2; ++k) {
                const int i = tid + 256 * k;
                const int pw = i >> 7, pg = (i >> 5) & 3, token = (i >> 2) & 7, r = i & 3;
                unsigned char* qk = lds + (pj ? L_K : L_Q) + token * QK_STRIDE;
                *(u16*)(qk + ((2 * pw) * 16 + 4 * pg + r) * 2) = (u16)v[k];
                *(u16*)(qk + ((2 * pw + 1) * 16 + 4 * pg + r) * 2) = (u16)(v[k] >> 16);
                *(u16*)(lds + L_VT + (pj * 64 + pw * 16 + 4 * pg + r) * 16 + token * 2) = (u16)(v[k] >> 32);
            }
        }
        __syncthreads();
        f32x4 st[3];
#pragma unroll
        for (int t = 0; t < 3; ++t) st[t] = (f32x4){0.f, 0.f, 0.f, 0.f};
#pragma unroll
        for (int ks = 0; ks < 4; ++ks) {
            const bf16x8 qf = *(const bf16x8*)(lds + L_Q + (c16 & 7) * QK_STRIDE + ks * 64 + g * 16);
            const bf16x8 kf = *(const bf16x8*)(lds + L_K + (c16 & 7) * QK_STRIDE + ks * 64 + g * 16);
            st[0] = mfma32(kf, qf, st[0]);
            st[1] = mfma32(kx[0][ks], qf, st[1]);
            st[2] = mfma32(kx[1][ks], qf, st[2]);
        }
        float x[3][4];
        float mx = -INFINITY;
#pragma unroll
        for (int t = 0; t < 3; ++t)
#pragma unroll
            for (int r = 0; r < 4; ++r) {
                float v = rbf(rbf(st[t][r]) * scale);
                if (t == 0 && g >= 2) v = -INFINITY;
                x[t][r] = v; mx = fmaxf(mx, v);
            }
        mx = fmaxf(mx, __shfl_xor(mx, 16)); mx = fmaxf(mx, __shfl_xor(mx, 32));
        float sum = 0.f;
#pragma unroll
        for (int t = 0; t < 3; ++t)
#pragma unroll
            for (int r = 0; r < 4; ++r) { x[t][r] = __expf(x[t][r] - mx); sum += x[t][r]; }
        sum += __shfl_xor(sum, 16); sum += __shfl_xor(sum, 32);
        const float inv = 1.0f / sum;
        bf16x8 px, pm;
#pragma unroll
        for (int r = 0; r < 4; ++r) {
            pm[r] = (short)f2bf(x[0][r] * inv); pm[4 + r] = 0;
            px[r] = (short)f2bf(x[1][r] * inv);
            px[4 + r] = (short)f2bf(x[2][r] * inv);
        }
        bf16x8 of[4];
#pragma unroll
        for (int et = 0; et < 8; ++et) {
            bf16x4 vm4 = (bf16x4){0, 0, 0, 0};
            if (g < 2) vm4 = *(const bf16x4*)(lds + L_VT + (et * 16 + c16) * 16 + g * 8);
            const bf16x8 vm = __builtin_shufflevector(vm4, (bf16x4){0, 0, 0, 0}, 0, 1, 2, 3, 4, 5, 6, 7);
            f32x4 o = mfma32(vx[et], px, (f32x4){0.f, 0.f, 0.f, 0.f});
            o = mfma32(vm, pm, o);
#pragma unroll
            for (int r = 0; r < 4; ++r) of[et >> 1][(et & 1) * 4 + r] = (short)f2bf(o[r]);
        }
        {
            gu64* xb = (gu64*)a.xb + (size_t)par * 32 * 5120 + (size_t)(h * 1280 + j * 640 + w * 160 + g * 8 + (c16 & 7)) * 4;
#pragma unroll
            for (int nt = 0; nt < 5; ++nt) {
                f32x4 p = (f32x4){0.f, 0.f, 0.f, 0.f};
#pragma unroll
                for (int ks = 0; ks < 4; ++ks) p = mfma32(wo[nt][ks], of[ks], p);
                if (c16 < 8) {
#pragma unroll
                    for (int r = 0; r < 4; ++r) {
                        const float pv = p[r];
                        __hip_atomic_store(xb + nt * 128 + r, (unsigned long long)__float_as_uint(pv) | ((unsigned long long)tag << 32), RLX_AGENT);
                    }
                }
            }
        }
        {
            const int hh0 = tid / 80, rem = tid - hh0 * 80;
            gu64* src = (gu64*)a.xb + (size_t)par * 32 * 5120 + (size_t)hh0 * 5120 + cu * 80 + rem;
            const int nk = (tid >= 240) ? 0 : (hh0 == 2 ? 10 : 11);
            unsigned long long v[11];
#pragma unroll
            for (int k = 0; k < 11; ++k) v[k] = (k < nk) ? __hip_atomic_load(src + (size_t)k * 15360, RLX_AGENT) : ((unsigned long long)tag << 32);
            for (unsigned spins = 0;; ++spins) {
                bool ok = true;
#pragma unroll
                for (int k = 0; k < 11; ++k) ok &= ((unsigned)(v[k] >> 32) == tag);
                if (__all(ok)) break;
                if (spins > kSpinCap) { __hip_atomic_store(tmo, 1u, RLX_AGENT); break; }
                if ((spins & 63u) == 63u && __hip_atomic_load(tmo, RLX_AGENT) != 0u) break;
#pragma unroll
                for (int k = 0; k < 11; ++k)
                    if ((unsigned)(v[k] >> 32) != tag) v[k] = __hip_atomic_load(src + (size_t)k * 15360, RLX_AGENT);
            }
#pragma unroll
            for (int k = 0; k < 11; ++k)
                if (k < nk) *(unsigned*)(lds + L_RED + ((hh0 + 3 * k) * 80 + rem) * 4) = (unsigned)v[k];
        }
        __syncthreads();
        if (tid < 128) {
            float sacc = 0.f;
            if (tid < 80) {
#pragma unroll
                for (int hh = 0; hh < 32; ++hh) sacc += *(const float*)(lds + L_RED + (hh * 80 + tid) * 4);
            }
            const unsigned mine = f2bf(sacc);
            const unsigned other = (unsigned)__shfl_xor((int)mine, 1);
            if (tid < 80 && (tid & 1) == 0)
                __hip_atomic_store((gu64*)a.xc + (size_t)par * 2560 + cu * 40 + (tid >> 1),
                                   (unsigned long long)(mine | (other << 16)) | ((unsigned long long)tag << 32), RLX_AGENT);
        }
    }
}

struct AttnArgs { const u16* Q; const u16* K; const u16* Vt; const u16* Km; const u16* Vmt; u16* O; const u16* Bqkv; u16* Ob; };

__global__ __launch_bounds__(256) void attn_kernel(AttnArgs a) {
    const int tid = threadIdx.x, w = tid >> 6, lane = tid & 63, c16 = lane & 15, g = lane >> 4;
    if (blockIdx.x < 512) {
        const int item = blockIdx.x * 4 + w, s = item >> 5, h = item & 31;
        const float scale = 0.08838834764831845f;
        f32x4 st[3][2];
#pragma unroll
        for (int t = 0; t < 3; ++t) { st[t][0] = (f32x4){0.f, 0.f, 0.f, 0.f}; st[t][1] = (f32x4){0.f, 0.f, 0.f, 0.f}; }
#pragma unroll
        for (int ks = 0; ks < 4; ++ks) {
            const int eo = h * DH + 32 * ks + 8 * g;
            const bf16x8 q0 = *(const bf16x8*)(a.Q + (size_t)(s * SEG + c16) * HD + eo);
            const bf16x8 q1 = *(const bf16x8*)(a.Q + (size_t)(s * SEG + 16 + c16) * HD + eo);
            const bf16x8 k0 = *(const bf16x8*)(a.Km + (size_t)(s * MEMN + (c16 & 7)) * HD + eo);
            const bf16x8 k1 = *(const bf16x8*)(a.K + (size_t)(s * SEG + c16) * HD + eo);
            const bf16x8 k2 = *(const bf16x8*)(a.K + (size_t)(s * SEG + 16 + c16) * HD + eo);
            st[0][0] = mfma32(k0, q0, st[0][0]); st[0][1] = mfma32(k0, q1, st[0][1]);
            st[1][0] = mfma32(k1, q0, st[1][0]); st[1][1] = mfma32(k1, q1, st[1][1]);
            st[2][0] = mfma32(k2, q0, st[2][0]); st[2][1] = mfma32(k2, q1, st[2][1]);
        }
        bf16x8 px[2], pm[2];
#pragma unroll
        for (int qt = 0; qt < 2; ++qt) {
            const int qi = qt * 16 + c16;
            float x[3][4]; float mx = -INFINITY;
#pragma unroll
            for (int t = 0; t < 3; ++t)
#pragma unroll
                for (int r = 0; r < 4; ++r) {
                    float v = rbf(rbf(st[t][qt][r]) * scale);
                    const int key = (t - 1) * 16 + 4 * g + r;
                    const bool valid = (t == 0) ? (g < 2) : (key <= qi);
                    if (!valid) v = -INFINITY;
                    x[t][r] = v; mx = fmaxf(mx, v);
                }
            mx = fmaxf(mx, __shfl_xor(mx, 16)); mx = fmaxf(mx, __shfl_xor(mx, 32));
            float sum = 0.f;
#pragma unroll
            for (int t = 0; t < 3; ++t)
#pragma unroll
                for (int r = 0; r < 4; ++r) { x[t][r] = __expf(x[t][r] - mx); sum += x[t][r]; }
            sum += __shfl_xor(sum, 16); sum += __shfl_xor(sum, 32);
            const float inv = 1.0f / sum;
#pragma unroll
            for (int r = 0; r < 4; ++r) {
                pm[qt][r] = (short)f2bf(x[0][r] * inv); pm[qt][4 + r] = 0;
                px[qt][r] = (short)f2bf(x[1][r] * inv);
                px[qt][4 + r] = (short)f2bf(x[2][r] * inv);
            }
        }
#pragma unroll
        for (int et = 0; et < 8; ++et) {
            const int e = h * DH + et * 16 + c16;
            const u16* p = a.Vt + ((size_t)s * HD + e) * 32;
            const bf16x4 lo = *(const bf16x4*)(p + 4 * g), hi = *(const bf16x4*)(p + 16 + 4 * g);
            const bf16x8 vx = __builtin_shufflevector(lo, hi, 0, 1, 2, 3, 4, 5, 6, 7);
            bf16x4 vm4 = (bf16x4){0, 0, 0, 0};
            if (g < 2) vm4 = *(const bf16x4*)(a.Vmt + ((size_t)s * HD + e) * 8 + 4 * g);
            const bf16x8 vm = __builtin_shufflevector(vm4, (bf16x4){0, 0, 0, 0}, 0, 1, 2, 3, 4, 5, 6, 7);
#pragma unroll
            for (int qt = 0; qt < 2; ++qt) {
                f32x4 o = mfma32(vx, px[qt], (f32x4){0.f, 0.f, 0.f, 0.f});
                o = mfma32(vm, pm[qt], o);
                v2u pk;
                pk.x = (unsigned)f2bf(o[0]) | ((unsigned)f2bf(o[1]) << 16);
                pk.y = (unsigned)f2bf(o[2]) | ((unsigned)f2bf(o[3]) << 16);
                *(v2u*)(a.O + (size_t)(s * SEG + qt * 16 + c16) * HD + h * DH + et * 16 + 4 * g) = pk;
            }
        }
    } else {
        const int idx = (blockIdx.x - 512) * 256 + tid;
        const int h = idx & 31, pos = idx >> 5;
        const float scale = 0.22360679774997896f;
        const u16* qp = a.Bqkv + (size_t)pos * BD3 + h * BDH;
        float q[BDH];
#pragma unroll
        for (int d = 0; d < BDH; d += 2) { const unsigned u = *(const unsigned*)(qp + d); q[d] = bf2f((u16)u); q[d + 1] = bf2f((u16)(u >> 16)); }
        float lg[8]; float mx = -INFINITY;
#pragma unroll
        for (int wi = 0; wi < 8; ++wi) {
            const int kp = pos - 2 * wi;
            float v = -INFINITY;
            if (kp >= 0) {
                const u16* kpn = a.Bqkv + (size_t)kp * BD3 + DM + h * BDH;
                float d0 = 0.f;
#pragma unroll
                for (int d = 0; d < BDH; d += 2) { const unsigned u = *(const unsigned*)(kpn + d); d0 += q[d] * bf2f((u16)u); d0 += q[d + 1] * bf2f((u16)(u >> 16)); }
                v = rbf(rbf(d0) * scale);
            }
            lg[wi] = v; mx = fmaxf(mx, v);
        }
        float sum = 0.f;
#pragma unroll
        for (int wi = 0; wi < 8; ++wi) { lg[wi] = __expf(lg[wi] - mx); sum += lg[wi]; }
        const float inv = 1.0f / sum;
        float o[BDH];
#pragma unroll
        for (int d = 0; d < BDH; ++d) o[d] = 0.f;
#pragma unroll
        for (int wi = 0; wi < 8; ++wi) {
            const int kp = pos - 2 * wi;
            if (kp >= 0) {
                const float p = rbf(lg[wi] * inv);
                const u16* vpn = a.Bqkv + (size_t)kp * BD3 + 2 * DM + h * BDH;
#pragma unroll
                for (int d = 0; d < BDH; d += 2) { const unsigned u = *(const unsigned*)(vpn + d); o[d] += p * bf2f((u16)u); o[d + 1] += p * bf2f((u16)(u >> 16)); }
            }
        }
        u16* op = a.Ob + (size_t)pos * DM + h * BDH;
#pragma unroll
        for (int d = 0; d < BDH; d += 2) *(unsigned*)(op + d) = (unsigned)f2bf(o[d]) | ((unsigned)f2bf(o[d + 1]) << 16);
    }
}

extern "C" void kernel_launch(void* const* d_in, const int* in_sizes, int n_in,
                              void* d_out, int out_size, void* d_ws, size_t ws_size,
                              hipStream_t stream) {
    const u16* Ax = (const u16*)d_in[0];
    const u16* mem0 = (const u16*)d_in[1];
    const u16* Wq = (const u16*)d_in[2];
    const u16* Wk = (const u16*)d_in[3];
    const u16* Wv = (const u16*)d_in[4];
    const u16* Wo = (const u16*)d_in[5];
    const u16* Bx = (const u16*)d_in[7];
    const u16* Bw = (const u16*)d_in[8];
    const u16* Bwo = (const u16*)d_in[9];
    unsigned char* ws = (unsigned char*)d_ws;
    u16* out = (u16*)d_out;

    (void)hipMemsetAsync(ws, 0, XCHG_BYTES, stream);

    Gemm1Args g1{Ax, Wq, Wk, Wv, Bx, Bw, (u16*)(ws + Q_OFF), (u16*)(ws + K_OFF), (u16*)(ws + VT_OFF), (u16*)(ws + BQKV_OFF)};
    gemm1_kernel<<<16 * 96 + 16 * 15, 256, 0, stream>>>(g1);

    ChainArgs ca{mem0, Wq, Wk, Wv, Wo, (const u16*)(ws + K_OFF), (const u16*)(ws + VT_OFF), (u16*)(ws + KM_OFF), (u16*)(ws + VMT_OFF),
                 (unsigned long long*)(ws + XA_OFF), (unsigned long long*)(ws + XB_OFF), (unsigned long long*)(ws + XC_OFF), (unsigned*)(ws + TMO_OFF)};
    chain_kernel<<<64, 256, 0, stream>>>(ca);

    AttnArgs aa{(const u16*)(ws + Q_OFF), (const u16*)(ws + K_OFF), (const u16*)(ws + VT_OFF), (const u16*)(ws + KM_OFF), (const u16*)(ws + VMT_OFF),
                (u16*)(ws + O_OFF), (const u16*)(ws + BQKV_OFF), (u16*)(ws + OB_OFF)};
    attn_kernel<<<512 + 256, 256, 0, stream>>>(aa);

    Gemm2Args g2{(const u16*)(ws + O_OFF), Wo, (const u16*)(ws + OB_OFF), Bwo, out};
    gemm2_kernel<<<160, 256, 0, stream>>>(g2);
}
```

```cpp
#include <hip/hip_runtime.h>
#include <hip/hip_bf16.h>
#include <stdint.h>

#define GAS __attribute__((address_space(1)))
#define LAS __attribute__((address_space(3)))
typedef unsigned short u16;
typedef short bf16x8 __attribute__((ext_vector_type(8)));
typedef short bf16x4 __attribute__((ext_vector_type(4)));
typedef float f32x4 __attribute__((ext_vector_type(4)));
typedef unsigned v4u __attribute__((ext_vector_type(4)));
typedef unsigned v2u __attribute__((ext_vector_type(2)));
typedef GAS unsigned long long gu64;
typedef GAS unsigned gu32;
typedef LAS bf16x4 lds_bf16x4;
#define RLX_AGENT __ATOMIC_RELAXED, __HIP_MEMORY_SCOPE_AGENT

constexpr int SEQ = 2048, DM = 640, NH = 32, DH = 128, MEMN = 8, SEG = 32, NSEG = 64, HD = 4096;
constexpr int BD3 = 1920, BDH = 20;

constexpr size_t XA_OFF = 0;
constexpr size_t XA_BYTES = (size_t)2 * 64 * 512 * 8;
constexpr size_t XB_OFF = XA_OFF + XA_BYTES;
constexpr size_t XB_BYTES = (size_t)2 * 64 * 2560 * 8;
constexpr size_t XC_OFF = XB_OFF + XB_BYTES;
constexpr int XC_REP = 8;
constexpr size_t XC_BYTES = (size_t)2 * XC_REP * 2560 * 8;
constexpr size_t TMO_OFF = XC_OFF + XC_BYTES;
constexpr size_t FL_OFF = TMO_OFF + 256;
constexpr int F_KV = 0, F_Q = F_KV + 512, F_KM = F_Q + 512, F_ATT = F_KM + 2048, F_BQ = F_ATT + 64, F_TR = F_BQ + 16, F_END = F_TR + 1;
constexpr size_t XCHG_BYTES = (FL_OFF + (size_t)F_END * 64 + 255) / 256 * 256;
constexpr size_t Q_OFF = (XCHG_BYTES + 4095) / 4096 * 4096;
constexpr size_t K_OFF = Q_OFF + (size_t)SEQ * HD * 2;
constexpr size_t VT_OFF = K_OFF + (size_t)SEQ * HD * 2;
constexpr size_t KM_OFF = VT_OFF + (size_t)SEQ * HD * 2;
constexpr size_t VM_OFF = KM_OFF + (size_t)NSEG * MEMN * HD * 2;
constexpr size_t O_OFF = VM_OFF + (size_t)NSEG * MEMN * HD * 2;
constexpr size_t BQKV_OFF = O_OFF + (size_t)SEQ * HD * 2;
constexpr size_t WOT_OFF = BQKV_OFF + (size_t)SEQ * BD3 * 2;
constexpr size_t BWOT_OFF = WOT_OFF + (size_t)DM * HD * 2;
constexpr size_t WS_END = BWOT_OFF + (size_t)DM * DM * 2;

__device__ __forceinline__ int opaque_tid() { int t = threadIdx.x; asm volatile("" : "+v"(t)); return t; }
__device__ __forceinline__ u16 f2bf(float f) { return __builtin_bit_cast(u16, (__bf16)f); }
__device__ __forceinline__ float bf2f(u16 b) { return __builtin_bit_cast(float, (unsigned)b << 16); }
__device__ __forceinline__ float rbf(float f) { return bf2f(f2bf(f)); }
__device__ __forceinline__ f32x4 mfma32(bf16x8 a, bf16x8 b, f32x4 c) { return __builtin_amdgcn_mfma_f32_16x16x32_bf16(a, b, c, 0, 0, 0); }

__device__ __forceinline__ void st8(u16* p, v2u v) { __hip_atomic_store((gu64*)p, (unsigned long long)v.x | ((unsigned long long)v.y << 32), RLX_AGENT); }
__device__ __forceinline__ unsigned off_b(unsigned row, unsigned ch) { return 256u * row + 16u * (ch ^ (((row & 3) << 2) | ((row >> 2) & 3))); }

template <int MODE>
__device__ __forceinline__ void gemm_tile(const u16* __restrict__ A, int lda, const u16* __restrict__ W, int ldw,
                                          u16* __restrict__ C, int ldc, int K, int m0, int n0, unsigned char* lds) {
    const int tid = opaque_tid(), w = tid >> 6, lane = tid & 63, c16 = lane & 15, g = lane >> 4;
    const int wm = w >> 1, wn = w & 1;
    unsigned char* LA = lds;
    unsigned char* LW = lds + 32768;
    f32x4 acc[4][4];
#pragma unroll
    for (int i = 0; i < 4; ++i)
#pragma unroll
        for (int jn = 0; jn < 4; ++jn) acc[i][jn] = (f32x4){0.f, 0.f, 0.f, 0.f};
    v4u ra[4], rw[4];
    auto gload = [&](int kt) {
#pragma unroll
        for (int i = 0; i < 4; ++i) {
            const int c = tid + 256 * i;
            ra[i] = *(const v4u*)(A + (size_t)(m0 + (c >> 3)) * lda + kt * 64 + (c & 7) * 8);
            rw[i] = *(const v4u*)(W + (size_t)(kt * 64 + (c >> 4)) * ldw + n0 + (c & 15) * 8);
        }
    };
    auto lstore = [&](int buf) {
#pragma unroll
        for (int i = 0; i < 4; ++i) {
            const int c = tid + 256 * i;
            const int row = c >> 3, ch = c & 7;
            *(v4u*)(LA + buf * 16384 + row * 128 + 16 * (ch ^ (row & 7))) = ra[i];
            const int r2 = c >> 4, ch2 = c & 15;
            *(v4u*)(LW + buf * 16384 + (r2 >> 5) * 8192 + off_b(r2 & 31, ch2)) = rw[i];
        }
    };
    const int nk = K / 64;
    gload(0); lstore(0); __syncthreads();
    const unsigned q4 = (lane & 15) >> 2, p4 = lane & 3;
#pragma unroll 1
    for (int kt = 0; kt < nk; ++kt) {
        const int buf = kt & 1;
        if (kt + 1 < nk) gload(kt + 1);
#pragma unroll
        for (int ks = 0; ks < 2; ++ks) {
            bf16x8 af[4], wf[4];
#pragma unroll
            for (int mt = 0; mt < 4; ++mt) {
                const int row = wm * 64 + mt * 16 + c16, ch = ks * 4 + g;
                af[mt] = *(const bf16x8*)(LA + buf * 16384 + row * 128 + 16 * (ch ^ (row & 7)));
            }
#pragma unroll
            for (int nt = 0; nt < 4; ++nt) {
                const unsigned cb = wn * 4 + nt;
                unsigned char* base = LW + buf * 16384 + ks * 8192;
                const bf16x4 lo = __builtin_amdgcn_ds_read_tr16_b64_v4i16((lds_bf16x4*)(base + off_b(8 * g + q4, 2 * cb + (p4 >> 1)) + 8 * (p4 & 1)));
                const bf16x4 hi = __builtin_amdgcn_ds_read_tr16_b64_v4i16((lds_bf16x4*)(base + off_b(8 * g + 4 + q4, 2 * cb + (p4 >> 1)) + 8 * (p4 & 1)));
                wf[nt] = __builtin_shufflevector(lo, hi, 0, 1, 2, 3, 4, 5, 6, 7);
            }
#pragma unroll
            for (int mt = 0; mt < 4; ++mt)
#pragma unroll
                for (int nt = 0; nt < 4; ++nt)
                    acc[mt][nt] = (MODE == 0) ? mfma32(wf[nt], af[mt], acc[mt][nt]) : mfma32(af[mt], wf[nt], acc[mt][nt]);
        }
        if (kt + 1 < nk) lstore(buf ^ 1);
        __syncthreads();
    }
#pragma unroll
    for (int mt = 0; mt < 4; ++mt)
#pragma unroll
        for (int nt = 0; nt < 4; ++nt) {
            v2u pk;
            pk.x = (unsigned)f2bf(acc[mt][nt][0]) | ((unsigned)f2bf(acc[mt][nt][1]) << 16);
            pk.y = (unsigned)f2bf(acc[mt][nt][2]) | ((unsigned)f2bf(acc[mt][nt][3]) << 16);
            if (MODE == 0) {
                const int m = m0 + wm * 64 + mt * 16 + c16, n = n0 + wn * 64 + nt * 16 + 4 * g;
                st8(C + (size_t)m * ldc + n, pk);
            } else {
                const int m = m0 + wm * 64 + mt * 16 + 4 * g, n = n0 + wn * 64 + nt * 16 + c16;
                st8(C + ((size_t)(m >> 5) * HD + n) * 32 + 8 * g + 4 * ((m >> 4) & 1), pk);
            }
        }
}

struct Args { const u16* Ax; const u16* mem0; const u16* Wq; const u16* Wk; const u16* Wv; const u16* Wo;
              const u16* Bx; const u16* Bw; const u16* Bwo; u16* out; unsigned char* ws; };

constexpr unsigned kSpinCap = 1u << 17;

typedef __amdgpu_buffer_rsrc_t rsrc_t;
__device__ __forceinline__ void st16(rsrc_t rs, int off, v4u v) { __builtin_amdgcn_raw_buffer_store_b128(v, rs, off, 0, 16); }
template <int NPT, bool TAG16>
__device__ __forceinline__ void gather(rsrc_t rs, int off, int str, unsigned tag, v4u (&v)[NPT], gu32* tmo) {
    for (unsigned spins = 0;; ++spins) {
        asm volatile("" ::: "memory");
#pragma unroll
        for (int k = 0; k < NPT; ++k) v[k] = __builtin_amdgcn_raw_buffer_load_b128(rs, off + k * str, 0, 16);
        bool ok = true;
#pragma unroll
        for (int k = 0; k < NPT; ++k) {
            if (TAG16) ok &= ((v[k].y >> 16) == tag) & ((v[k].w >> 16) == tag);
            else ok &= (v[k].y == tag) & (v[k].w == tag);
        }
        if (__all(ok)) break;
        if ((spins & 63u) == 63u) {
            if (spins > kSpinCap) { __hip_atomic_store(tmo, 1u, RLX_AGENT); break; }
            if (__hip_atomic_load(tmo, RLX_AGENT) != 0u) break;
        }
    }
}
__device__ __forceinline__ float xmax16_32(float v) {
    auto r = __builtin_amdgcn_permlane16_swap(__float_as_uint(v), __float_as_uint(v), false, false);
    v = fmaxf(__uint_as_float(r[0]), __uint_as_float(r[1]));
    auto r2 = __builtin_amdgcn_permlane32_swap(__float_as_uint(v), __float_as_uint(v), false, false);
    return fmaxf(__uint_as_float(r2[0]), __uint_as_float(r2[1]));
}
__device__ __forceinline__ float xsum16_32(float v) {
    auto r = __builtin_amdgcn_permlane16_swap(__float_as_uint(v), __float_as_uint(v), false, false);
    v = __uint_as_float(r[0]) + __uint_as_float(r[1]);
    auto r2 = __builtin_amdgcn_permlane32_swap(__float_as_uint(v), __float_as_uint(v), false, false);
    return __uint_as_float(r2[0]) + __uint_as_float(r2[1]);
}

constexpr int MEM_STRIDE = 1296, QK_STRIDE = 272;
constexpr int L_WO = 0;
constexpr int L_MEM = L_WO + 81920, L_Q = L_MEM + 8 * MEM_STRIDE, L_K = L_Q + 8 * QK_STRIDE, L_VT = L_K + 8 * QK_STRIDE,
              L_RED = L_VT + 128 * 32, L_END = L_RED + 6 * 80 * 4;
constexpr int L_STAGE = L_MEM;
constexpr int CHAIN_LDS = (L_STAGE + 51200 > L_END) ? (L_STAGE + 51200) : L_END;

__device__ __forceinline__ gu32* flagp(unsigned char* ws, int idx) { return (gu32*)(ws + FL_OFF + (size_t)idx * 64); }

__device__ __forceinline__ void chain_role(const Args& a, unsigned char* lds) {
    const int cu = blockIdx.x, h = cu >> 1, j = cu & 1;
    const int tid = threadIdx.x, w = tid >> 6, lane = tid & 63, c16 = lane & 15, g = lane >> 4;
    gu32* tmo = (gu32*)(a.ws + TMO_OFF);
    const rsrc_t xr = __builtin_amdgcn_make_buffer_rsrc((void*)a.ws, 0, (int)TMO_OFF, 0x00020000);
    const rsrc_t kr_ = __builtin_amdgcn_make_buffer_rsrc((void*)(a.ws + K_OFF), 0, SEQ * HD * 2, 0x00020000);
    const rsrc_t vr_ = __builtin_amdgcn_make_buffer_rsrc((void*)(a.ws + VT_OFF), 0, SEQ * HD * 2, 0x00020000);
    u16* Km = (u16*)(a.ws + KM_OFF);
    u16* Vm = (u16*)(a.ws + VM_OFF);

    bf16x8 wqkv[3][20];
    {
        unsigned char* stg = lds + L_STAGE;
        const u16* Wqk = j ? a.Wk : a.Wq;
        const unsigned q4 = (lane & 15) >> 2, p4 = lane & 3;
#pragma unroll
        for (int rd = 0; rd < 5; ++rd) {
            v4u tmp[12];
#pragma unroll
            for (int i = 0; i < 12; ++i) {
                const int c = tid + 256 * i;
                const int row = c / 24, ch = c - row * 24;
                const size_t kr = (size_t)(rd * 128 + row) * HD + h * DH;
                tmp[i] = (ch < 16) ? *(const v4u*)(Wqk + kr + ch * 8) : *(const v4u*)(a.Wv + kr + j * 64 + (ch - 16) * 8);
            }
#pragma unroll
            for (int i = 0; i < 12; ++i) {
                const int c = tid + 256 * i;
                const int row = c / 24, ch = c - row * 24;
                *(v4u*)(stg + row * 400 + ch * 16) = tmp[i];
            }
            __syncthreads();
#pragma unroll
            for (int k4 = 0; k4 < 4; ++k4)
#pragma unroll
                for (int t = 0; t < 3; ++t) {
                    const int col = (t < 2) ? ((2 * w + t) * 16) : (128 + w * 16);
                    unsigned char* p0 = stg + (k4 * 32 + 8 * g + q4) * 400 + (col + 4 * p4) * 2;
                    const bf16x4 lo = __builtin_amdgcn_ds_read_tr16_b64_v4i16((lds_bf16x4*)p0);
                    const bf16x4 hi = __builtin_amdgcn_ds_read_tr16_b64_v4i16((lds_bf16x4*)(p0 + 4 * 400));
                    wqkv[t][rd * 4 + k4] = __builtin_shufflevector(lo, hi, 0, 1, 2, 3, 4, 5, 6, 7);
                }
            __syncthreads();
        }
#pragma unroll
        for (int rd = 0; rd < 2; ++rd) {
            v4u tmp[10];
#pragma unroll
            for (int i = 0; i < 10; ++i) {
                const int c = tid + 256 * i;
                const int row = c / 40, ch = c - row * 40;
                tmp[i] = *(const v4u*)(a.Wo + (size_t)(h * DH + rd * 64 + row) * DM + j * 320 + ch * 8);
            }
#pragma unroll
            for (int i = 0; i < 10; ++i) {
                const int c = tid + 256 * i;
                const int row = c / 40, ch = c - row * 40;
                *(v4u*)(stg + row * 656 + ch * 16) = tmp[i];
            }
            __syncthreads();
#pragma unroll
            for (int k2 = 0; k2 < 2; ++k2)
#pragma unroll
                for (int nt = 0; nt < 5; ++nt) {
                    unsigned char* p0 = stg + (k2 * 32 + 4 * g + q4) * 656 + (w * 80 + nt * 16 + 4 * p4) * 2;
                    const bf16x4 lo = __builtin_amdgcn_ds_read_tr16_b64_v4i16((lds_bf16x4*)p0);
                    const bf16x4 hi = __builtin_amdgcn_ds_read_tr16_b64_v4i16((lds_bf16x4*)(p0 + 16 * 656));
                    *(bf16x8*)(lds + L_WO + ((w * 5 + nt) * 4 + rd * 2 + k2) * 1024 + lane * 16) = __builtin_shufflevector(lo, hi, 0, 1, 2, 3, 4, 5, 6, 7);
                }
            __syncthreads();
        }
        if (tid < 128) *(v4u*)(lds + L_VT + tid * 32 + 16) = (v4u){0u, 0u, 0u, 0u};
    }
    const float scale = 0.08838834764831845f;
    const int tt = tid < 240 ? tid : 239, hg = tt / 40, pr = tt - hg * 40;

    for (int s = 0; s < NSEG; ++s) {
        const unsigned tag = (unsigned)(s + 1);
        const int par = s & 1;
        if ((s & 3) == 0) {
            gu32* fl = flagp(a.ws, F_KV + (s >> 2) * 32 + h);
            for (unsigned spins = 0;; ++spins) {
                if (__builtin_amdgcn_readfirstlane(__hip_atomic_load(fl, RLX_AGENT)) >= 2u) break;
                __builtin_amdgcn_s_sleep(4);
                if ((spins & 63u) == 63u) {
                    if (spins > kSpinCap) { __hip_atomic_store(tmo, 1u, RLX_AGENT); break; }
                    if (__hip_atomic_load(tmo, RLX_AGENT) != 0u) break;
                }
            }
            __builtin_amdgcn_fence(__ATOMIC_ACQUIRE, "wavefront");
        }
        bf16x8 kx[2][4], vx[8];
#pragma unroll
        for (int kt = 0; kt < 2; ++kt)
#pragma unroll
            for (int ks = 0; ks < 4; ++ks)
                kx[kt][ks] = __builtin_bit_cast(bf16x8, __builtin_amdgcn_raw_buffer_load_b128(kr_, ((s * SEG + kt * 16 + c16) * HD + h * DH + 32 * ks + 8 * g) * 2, 0, 16));
#pragma unroll
        for (int et = 0; et < 8; ++et)
            vx[et] = __builtin_bit_cast(bf16x8, __builtin_amdgcn_raw_buffer_load_b128(vr_, ((s * HD + h * DH + et * 16 + c16) * 32 + 8 * g) * 2, 0, 16));
        if (s == 0) {
            for (int i = tid; i < 8 * 80; i += 256) {
                const int tk = i / 80, ch = i % 80;
                *(v4u*)(lds + L_MEM + tk * MEM_STRIDE + ch * 16) = *(const v4u*)(a.mem0 + tk * DM + ch * 8);
            }
        } else {
            v4u v[5];
            __builtin_amdgcn_s_sleep(16);
            gather<5, false>(xr, (int)XC_OFF + (((s - 1) & 1) * XC_REP + (cu & (XC_REP - 1))) * 20480 + tid * 16, 4096, (unsigned)s, v, tmo);
            {
                const int l2 = (2 * tid) & 63;
                const int tok = l2 & 7, col = ((tid >> 5) * 16 + (l2 >> 4) * 4 + ((l2 >> 3) & 1) * 2) * 2;
#pragma unroll
                for (int k = 0; k < 5; ++k) {
                    *(unsigned*)(lds + L_MEM + tok * MEM_STRIDE + col + k * 256) = v[k].x;
                    *(unsigned*)(lds + L_MEM + (tok + 1) * MEM_STRIDE + col + k * 256) = v[k].z;
                }
            }
        }
        __syncthreads();
        f32x4 acc[3];
        {
            bf16x8 mb[20];
#pragma unroll
            for (int ks = 0; ks < 20; ++ks) mb[ks] = *(const bf16x8*)(lds + L_MEM + (c16 & 7) * MEM_STRIDE + ks * 64 + g * 16);
            __builtin_amdgcn_sched_barrier(0);
#pragma unroll
            for (int t = 0; t < 3; ++t) acc[t] = (f32x4){0.f, 0.f, 0.f, 0.f};
#pragma unroll
            for (int ks = 0; ks < 20; ++ks)
#pragma unroll
                for (int t = 0; t < 3; ++t) acc[t] = mfma32(wqkv[t][ks], mb[ks], acc[t]);
        }
        u16 qv[3][4];
#pragma unroll
        for (int t = 0; t < 3; ++t)
#pragma unroll
            for (int r = 0; r < 4; ++r) { const float f = acc[t][r]; qv[t][r] = f2bf(f); }
        if (s + 1 < NSEG) {
            const bool hi = c16 >= 8;
            v4u pk;
            pk.x = (unsigned)(hi ? qv[0][2] : qv[0][0]) | ((unsigned)(hi ? qv[1][2] : qv[1][0]) << 16);
            pk.y = (unsigned)(hi ? qv[2][2] : qv[2][0]) | (tag << 16);
            pk.z = (unsigned)(hi ? qv[0][3] : qv[0][1]) | ((unsigned)(hi ? qv[1][3] : qv[1][1]) << 16);
            pk.w = (unsigned)(hi ? qv[2][3] : qv[2][1]) | (tag << 16);
            st16(xr, (int)XA_OFF + (par * 64 + cu) * 4096 + tid * 16, pk);
        }
        if (c16 < 8) {
            const int token = c16;
            unsigned char* qk = lds + (j ? L_K : L_Q) + token * QK_STRIDE;
#pragma unroll
            for (int t = 0; t < 2; ++t) {
                v2u pk; pk.x = (unsigned)qv[t][0] | ((unsigned)qv[t][1] << 16); pk.y = (unsigned)qv[t][2] | ((unsigned)qv[t][3] << 16);
                *(v2u*)(qk + ((2 * w + t) * 16 + 4 * g) * 2) = pk;
                if (j) st8(Km + (size_t)(s * MEMN + token) * HD + h * DH + (2 * w + t) * 16 + 4 * g, pk);
            }
            {
                v2u pk; pk.x = (unsigned)qv[2][0] | ((unsigned)qv[2][1] << 16); pk.y = (unsigned)qv[2][2] | ((unsigned)qv[2][3] << 16);
                st8(Vm + (size_t)(s * MEMN + token) * HD + h * DH + j * 64 + w * 16 + 4 * g, pk);
            }
#pragma unroll
            for (int r = 0; r < 4; ++r) {
                const int e = j * 64 + w * 16 + 4 * g + r;
                *(u16*)(lds + L_VT + e * 32 + token * 2) = qv[2][r];
            }
        }
        if (s + 1 == NSEG) {
            asm volatile("s_waitcnt vmcnt(0)" ::: "memory");
            __syncthreads();
            if (tid == 0) __hip_atomic_fetch_add(flagp(a.ws, F_KM + s * 32 + h), 1u, RLX_AGENT);
        }
        if (s + 1 == NSEG) break;
        {
            v4u v[1];
            gather<1, true>(xr, (int)XA_OFF + (par * 64 + (cu ^ 1)) * 4096 + tid * 16, 0, tag, v, tmo);
            const int pj = j ^ 1;
            const int pw = tid >> 6, pg = (tid >> 4) & 3, token = tid & 7, r = ((tid >> 3) & 1) * 2;
            unsigned char* qk = lds + (pj ? L_K : L_Q) + token * QK_STRIDE;
            *(unsigned*)(qk + ((2 * pw) * 16 + 4 * pg + r) * 2) = (v[0].x & 0xffffu) | (v[0].z << 16);
            *(unsigned*)(qk + ((2 * pw + 1) * 16 + 4 * pg + r) * 2) = (v[0].x >> 16) | (v[0].z & 0xffff0000u);
            *(u16*)(lds + L_VT + (pj * 64 + pw * 16 + 4 * pg + r) * 32 + token * 2) = (u16)v[0].y;
            *(u16*)(lds + L_VT + (pj * 64 + pw * 16 + 4 * pg + r + 1) * 32 + token * 2) = (u16)v[0].w;
        }
        asm volatile("s_waitcnt vmcnt(0)" ::: "memory");
        __syncthreads();
        if (tid == 0) __hip_atomic_fetch_add(flagp(a.ws, F_KM + s * 32 + h), 1u, RLX_AGENT);
        bf16x8 qf[4], kf[4];
        bf16x4 vm4[8];
#pragma unroll
        for (int ks = 0; ks < 4; ++ks) {
            qf[ks] = *(const bf16x8*)(lds + L_Q + (c16 & 7) * QK_STRIDE + ks * 64 + g * 16);
            kf[ks] = *(const bf16x8*)(lds + L_K + (c16 & 7) * QK_STRIDE + ks * 64 + g * 16);
        }
#pragma unroll
        for (int et = 0; et < 8; ++et) vm4[et] = *(const bf16x4*)(lds + L_VT + (et * 16 + c16) * 32 + g * 8);
        __builtin_amdgcn_sched_barrier(0);
        f32x4 st[3];
#pragma unroll
        for (int t = 0; t < 3; ++t) st[t] = (f32x4){0.f, 0.f, 0.f, 0.f};
#pragma unroll
        for (int ks = 0; ks < 4; ++ks) {
            st[1] = mfma32(kx[0][ks], qf[ks], st[1]);
            st[2] = mfma32(kx[1][ks], qf[ks], st[2]);
            st[0] = mfma32(kf[ks], qf[ks], st[0]);
        }
        __builtin_amdgcn_sched_barrier(0);
        bf16x8 wo[5][4];
#pragma unroll
        for (int nt = 0; nt < 5; ++nt)
#pragma unroll
            for (int ks = 0; ks < 4; ++ks) wo[nt][ks] = *(const bf16x8*)(lds + L_WO + ((w * 5 + nt) * 4 + ks) * 1024 + lane * 16);
        __builtin_amdgcn_sched_barrier(0);
        float x[3][4];
        float mx = -INFINITY;
#pragma unroll
        for (int t = 0; t < 3; ++t)
#pragma unroll
            for (int r = 0; r < 4; ++r) {
                const float sv = st[t][r];
                float v = rbf(rbf(sv) * scale);
                if (t == 0 && g >= 2) v = -INFINITY;
                x[t][r] = v; mx = fmaxf(mx, v);
            }
        mx = xmax16_32(mx);
        float sum = 0.f;
#pragma unroll
        for (int t = 0; t < 3; ++t)
#pragma unroll
            for (int r = 0; r < 4; ++r) { x[t][r] = __expf(x[t][r] - mx); sum += x[t][r]; }
        sum = xsum16_32(sum);
        const float inv = 1.0f / sum;
        bf16x8 px, pm;
#pragma unroll
        for (int r = 0; r < 4; ++r) {
            pm[r] = (short)f2bf(x[0][r] * inv); pm[4 + r] = 0;
            px[r] = (short)f2bf(x[1][r] * inv);
            px[4 + r] = (short)f2bf(x[2][r] * inv);
        }
        f32x4 oa[8];
#pragma unroll
        for (int et = 0; et < 8; ++et) oa[et] = mfma32(vx[et], px, (f32x4){0.f, 0.f, 0.f, 0.f});
#pragma unroll
        for (int et = 0; et < 8; ++et) {
            const bf16x8 vm = __builtin_shufflevector(vm4[et], (bf16x4){0, 0, 0, 0}, 0, 1, 2, 3, 4, 5, 6, 7);
            oa[et] = mfma32(vm, pm, oa[et]);
        }
        bf16x8 of[4];
#pragma unroll
        for (int et = 0; et < 8; ++et)
#pragma unroll
            for (int r = 0; r < 4; ++r) { const float f = oa[et][r]; of[et >> 1][(et & 1) * 4 + r] = (short)f2bf(f); }
        {
            f32x4 p[5];
#pragma unroll
            for (int nt = 0; nt < 5; ++nt) p[nt] = (f32x4){0.f, 0.f, 0.f, 0.f};
#pragma unroll
            for (int ks = 0; ks < 4; ++ks)
#pragma unroll
                for (int nt = 0; nt < 5; ++nt) p[nt] = mfma32(wo[nt][ks], of[ks], p[nt]);
            const int off = (int)XB_OFF + (par * 32 * 2560 + h * 2560 + (j * 4 + w) * 320 + lane) * 16;
            const bool hi = c16 >= 8;
#pragma unroll
            for (int nt = 0; nt < 5; ++nt) {
                const float p0 = p[nt][0], p1 = p[nt][1], p2 = p[nt][2], p3 = p[nt][3];
                v4u pk; pk.x = __float_as_uint(hi ? p2 : p0); pk.y = tag; pk.z = __float_as_uint(hi ? p3 : p1); pk.w = tag;
                st16(xr, off + nt * 1024, pk);
            }
        }
        {
            const int off = (int)XB_OFF + (par * 32 * 2560 + hg * 2560 + cu * 40 + pr) * 16;
            const int off5 = off + (hg < 2 ? 5 : 4) * 6 * 2560 * 16;
            v4u v[6];
            __builtin_amdgcn_s_sleep(16);
            for (unsigned spins = 0;; ++spins) {
                asm volatile("" ::: "memory");
#pragma unroll
                for (int k = 0; k < 5; ++k) v[k] = __builtin_amdgcn_raw_buffer_load_b128(xr, off + k * 6 * 2560 * 16, 0, 16);
                v[5] = __builtin_amdgcn_raw_buffer_load_b128(xr, off5, 0, 16);
                bool ok = true;
#pragma unroll
                for (int k = 0; k < 6; ++k) ok &= (v[k].y == tag) & (v[k].w == tag);
                if (__all(ok)) break;
                if ((spins & 63u) == 63u) {
                    if (spins > kSpinCap) { __hip_atomic_store(tmo, 1u, RLX_AGENT); break; }
                    if (__hip_atomic_load(tmo, RLX_AGENT) != 0u) break;
                }
            }
            float s0 = 0.f, s1 = 0.f;
#pragma unroll
            for (int k = 0; k < 5; ++k) { s0 += __uint_as_float(v[k].x); s1 += __uint_as_float(v[k].z); }
            if (hg < 2) { s0 += __uint_as_float(v[5].x); s1 += __uint_as_float(v[5].z); }
            if (tid < 240) { v2u pk; pk.x = __float_as_uint(s0); pk.y = __float_as_uint(s1); *(v2u*)(lds + L_RED + tid * 8) = pk; }
        }
        __syncthreads();
        if (tid < 128) {
            const int t80 = tid < 80 ? tid : 79;
            const float* rp = (const float*)(lds + L_RED) + t80;
            const float sacc = ((rp[0] + rp[80]) + (rp[160] + rp[240])) + (rp[320] + rp[400]);
            const unsigned mine = f2bf(sacc);
            const unsigned m1 = (unsigned)__builtin_amdgcn_mov_dpp((int)mine, 0x55, 0xf, 0xf, true);
            const unsigned m2 = (unsigned)__builtin_amdgcn_mov_dpp((int)mine, 0xAA, 0xf, 0xf, true);
            const unsigned m3 = (unsigned)__builtin_amdgcn_mov_dpp((int)mine, 0xFF, 0xf, 0xf, true);
            if (tid < 80 && (tid & 3) == 0) {
                v4u pk; pk.x = mine | (m1 << 16); pk.y = tag; pk.z = m2 | (m3 << 16); pk.w = tag;
#pragma unroll
                for (int rp = 0; rp < XC_REP; ++rp) st16(xr, (int)XC_OFF + (par * XC_REP + rp) * 20480 + (cu * 20 + (tid >> 2)) * 16, pk);
            }
        }
    }
}

__device__ __forceinline__ void wait_flags(gu32* myflag, unsigned target, bool active, gu32* tmo) {
    if (threadIdx.x < 64) {
        for (unsigned spins = 0;; ++spins) {
            const bool ok = !active || (__hip_atomic_load(myflag, RLX_AGENT) >= target);
            if (__all(ok)) break;
            __builtin_amdgcn_s_sleep(8);
            if ((spins & 63u) == 63u) {
                if (spins > kSpinCap) { __hip_atomic_store(tmo, 1u, RLX_AGENT); break; }
                if (__hip_atomic_load(tmo, RLX_AGENT) != 0u) break;
            }
        }
        __builtin_amdgcn_fence(__ATOMIC_ACQUIRE, "agent");
        asm volatile("s_waitcnt vmcnt(0)" ::: "memory");
    }
    __syncthreads();
}
__device__ __forceinline__ void signal_flag(gu32* flag) {
    asm volatile("s_waitcnt vmcnt(0)" ::: "memory");
    __syncthreads();
    if (threadIdx.x == 0) __hip_atomic_fetch_add(flag, 1u, RLX_AGENT);
}

__device__ __forceinline__ void transpose_tile(const u16* __restrict__ S, int lds_, u16* D, int ldd, int k0, int n0, unsigned char* lds, unsigned char* ws) {
    const int tid = opaque_tid();
    u16* T = (u16*)lds;
#pragma unroll
    for (int i = 0; i < 2; ++i) {
        const int c = tid + 256 * i, row = c >> 3, ch = c & 7;
        const v4u v = *(const v4u*)(S + (size_t)(k0 + row) * lds_ + n0 + ch * 8);
        const unsigned wv[4] = {v.x, v.y, v.z, v.w};
#pragma unroll
        for (int e = 0; e < 4; ++e) {
            T[(ch * 8 + 2 * e) * 72 + row] = (u16)wv[e];
            T[(ch * 8 + 2 * e + 1) * 72 + row] = (u16)(wv[e] >> 16);
        }
    }
    __syncthreads();
    const rsrc_t dr = __builtin_amdgcn_make_buffer_rsrc((void*)D, 0, DM * ldd * 2, 0x00020000);
#pragma unroll
    for (int i = 0; i < 2; ++i) {
        const int c = tid + 256 * i, n = c >> 3, ch = c & 7;
        const v4u v = *(const v4u*)(T + n * 72 + ch * 8);
        st16(dr, ((n0 + n) * ldd + k0 + ch * 8) * 2, v);
    }
}

__device__ __forceinline__ void attn_x(const Args& a, int s, int h) {
    const int lane = opaque_tid() & 63, c16 = lane & 15, g = lane >> 4;
    const u16* Q = (const u16*)(a.ws + Q_OFF); const u16* K = (const u16*)(a.ws + K_OFF); const u16* Vt = (const u16*)(a.ws + VT_OFF);
    const u16* Km = (const u16*)(a.ws + KM_OFF); const u16* Vm = (const u16*)(a.ws + VM_OFF); u16* O = (u16*)(a.ws + O_OFF);
    const float scale = 0.08838834764831845f;
    f32x4 st[3][2];
#pragma unroll
    for (int t = 0; t < 3; ++t) { st[t][0] = (f32x4){0.f, 0.f, 0.f, 0.f}; st[t][1] = (f32x4){0.f, 0.f, 0.f, 0.f}; }
#pragma unroll
    for (int ks = 0; ks < 4; ++ks) {
        const int eo = h * DH + 32 * ks + 8 * g;
        const bf16x8 q0 = *(const bf16x8*)(Q + (size_t)(s * SEG + c16) * HD + eo);
        const bf16x8 q1 = *(const bf16x8*)(Q + (size_t)(s * SEG + 16 + c16) * HD + eo);
        const bf16x8 k0 = *(const bf16x8*)(Km + (size_t)(s * MEMN + (c16 & 7)) * HD + eo);
        const bf16x8 k1 = *(const bf16x8*)(K + (size_t)(s * SEG + c16) * HD + eo);
        const bf16x8 k2 = *(const bf16x8*)(K + (size_t)(s * SEG + 16 + c16) * HD + eo);
        st[0][0] = mfma32(k0, q0, st[0][0]); st[0][1] = mfma32(k0, q1, st[0][1]);
        st[1][0] = mfma32(k1, q0, st[1][0]); st[1][1] = mfma32(k1, q1, st[1][1]);
        st[2][0] = mfma32(k2, q0, st[2][0]); st[2][1] = mfma32(k2, q1, st[2][1]);
    }
    bf16x8 px[2], pm[2];
#pragma unroll
    for (int qt = 0; qt < 2; ++qt) {
        const int qi = qt * 16 + c16;
        float x[3][4]; float mx = -INFINITY;
#pragma unroll
        for (int t = 0; t < 3; ++t)
#pragma unroll
            for (int r = 0; r < 4; ++r) {
                const float sv = st[t][qt][r];
                float v = rbf(rbf(sv) * scale);
                const int key = (t - 1) * 16 + 4 * g + r;
                const bool valid = (t == 0) ? (g < 2) : (key <= qi);
                if (!valid) v = -INFINITY;
                x[t][r] = v; mx = fmaxf(mx, v);
            }
        mx = xmax16_32(mx);
        float sum = 0.f;
#pragma unroll
        for (int t = 0; t < 3; ++t)
#pragma unroll
            for (int r = 0; r < 4; ++r) { x[t][r] = __expf(x[t][r] - mx); sum += x[t][r]; }
        sum = xsum16_32(sum);
        const float inv = 1.0f / sum;
#pragma unroll
        for (int r = 0; r < 4; ++r) {
            pm[qt][r] = (short)f2bf(x[0][r] * inv); pm[qt][4 + r] = 0;
            px[qt][r] = (short)f2bf(x[1][r] * inv);
            px[qt][4 + r] = (short)f2bf(x[2][r] * inv);
        }
    }
#pragma unroll
    for (int et = 0; et < 8; ++et) {
        const int e = h * DH + et * 16 + c16;
        const bf16x8 vx = *(const bf16x8*)(Vt + ((size_t)s * HD + e) * 32 + 8 * g);
        bf16x8 vm = (bf16x8){0, 0, 0, 0, 0, 0, 0, 0};
        if (g < 2) {
#pragma unroll
            for (int r = 0; r < 4; ++r) vm[r] = (short)Vm[(size_t)(s * MEMN + 4 * g + r) * HD + e];
        }
#pragma unroll
        for (int qt = 0; qt < 2; ++qt) {
            f32x4 o = mfma32(vx, px[qt], (f32x4){0.f, 0.f, 0.f, 0.f});
            o = mfma32(vm, pm[qt], o);
            const float o0 = o[0], o1 = o[1], o2 = o[2], o3 = o[3];
            v2u pk;
            pk.x = (unsigned)f2bf(o0) | ((unsigned)f2bf(o1) << 16);
            pk.y = (unsigned)f2bf(o2) | ((unsigned)f2bf(o3) << 16);
            st8(O + (size_t)(s * SEG + qt * 16 + c16) * HD + h * DH + et * 16 + 4 * g, pk);
        }
    }
}

__device__ __forceinline__ void oproj_task(const Args& a, int s, int ns, unsigned char* lds) {
    const int tid = opaque_tid(), w = tid >> 6, lane = tid & 63, c16 = lane & 15, g = lane >> 4;
    const u16* O = (const u16*)(a.ws + O_OFF) + (size_t)(s * SEG + c16) * HD + w * 1024 + 8 * g;
    const u16* Wt = (const u16*)(a.ws + WOT_OFF) + (size_t)(ns * 64 + c16) * HD + w * 1024 + 8 * g;
    f32x4 acc[4][2];
#pragma unroll
    for (int nt = 0; nt < 4; ++nt) { acc[nt][0] = (f32x4){0.f, 0.f, 0.f, 0.f}; acc[nt][1] = (f32x4){0.f, 0.f, 0.f, 0.f}; }
#pragma unroll 4
    for (int ks = 0; ks < 32; ++ks) {
        const bf16x8 o0 = *(const bf16x8*)(O + ks * 32), o1 = *(const bf16x8*)(O + (size_t)16 * HD + ks * 32);
#pragma unroll
        for (int nt = 0; nt < 4; ++nt) {
            const bf16x8 wf = *(const bf16x8*)(Wt + (size_t)nt * 16 * HD + ks * 32);
            acc[nt][0] = mfma32(wf, o0, acc[nt][0]);
            acc[nt][1] = mfma32(wf, o1, acc[nt][1]);
        }
    }
    f32x4* R = (f32x4*)lds;
#pragma unroll
    for (int nt = 0; nt < 4; ++nt) { R[(w * 8 + nt * 2) * 64 + lane] = acc[nt][0]; R[(w * 8 + nt * 2 + 1) * 64 + lane] = acc[nt][1]; }
    __syncthreads();
#pragma unroll
    for (int i = 0; i < 2; ++i) {
        const int tile = w * 2 + i, nt = tile >> 1, mt = tile & 1;
        const f32x4 v = (R[tile * 64 + lane] + R[(8 + tile) * 64 + lane]) + (R[(16 + tile) * 64 + lane] + R[(24 + tile) * 64 + lane]);
        const float v0 = v[0], v1 = v[1], v2 = v[2], v3 = v[3];
        v2u pk;
        pk.x = (unsigned)f2bf(v0) | ((unsigned)f2bf(v1) << 16);
        pk.y = (unsigned)f2bf(v2) | ((unsigned)f2bf(v3) << 16);
        *(v2u*)(a.out + (size_t)(s * SEG + mt * 16 + c16) * (2 * DM) + ns * 64 + nt * 16 + 4 * g) = pk;
    }
    __syncthreads();
}

__device__ __forceinline__ void bfinal_task(const Args& a, int rb32, unsigned char* lds) {
    const int tid = opaque_tid(), w = tid >> 6, lane = tid & 63, c16 = lane & 15, g = lane >> 4;
    const u16* Bq = (const u16*)(a.ws + BQKV_OFF);
    constexpr int OST = 1296;
    const float scale = 0.22360679774997896f;
    for (int it = 0; it < 4; ++it) {
        const int idx = it * 256 + tid, h = idx & 31, row = idx >> 5, pos = rb32 * 32 + row;
        const u16* qp = Bq + (size_t)pos * BD3 + h * BDH;
        float q[BDH];
#pragma unroll
        for (int d = 0; d < BDH; d += 2) { const unsigned u = *(const unsigned*)(qp + d); q[d] = bf2f((u16)u); q[d + 1] = bf2f((u16)(u >> 16)); }
        float lg[8]; float mx = -INFINITY;
#pragma unroll
        for (int wi = 0; wi < 8; ++wi) {
            const int kp = pos - 2 * wi;
            float v = -INFINITY;
            if (kp >= 0) {
                const u16* kpn = Bq + (size_t)kp * BD3 + DM + h * BDH;
                float d0 = 0.f;
#pragma unroll
                for (int d = 0; d < BDH; d += 2) { const unsigned u = *(const unsigned*)(kpn + d); d0 += q[d] * bf2f((u16)u); d0 += q[d + 1] * bf2f((u16)(u >> 16)); }
                v = rbf(rbf(d0) * scale);
            }
            lg[wi] = v; mx = fmaxf(mx, v);
        }
        float sum = 0.f;
#pragma unroll
        for (int wi = 0; wi < 8; ++wi) { lg[wi] = __expf(lg[wi] - mx); sum += lg[wi]; }
        const float inv = 1.0f / sum;
        float o[BDH];
#pragma unroll
        for (int d = 0; d < BDH; ++d) o[d] = 0.f;
#pragma unroll
        for (int wi = 0; wi < 8; ++wi) {
            const int kp = pos - 2 * wi;
            if (kp >= 0) {
                const float p = rbf(lg[wi] * inv);
                const u16* vpn = Bq + (size_t)kp * BD3 + 2 * DM + h * BDH;
#pragma unroll
                for (int d = 0; d < BDH; d += 2) { const unsigned u = *(const unsigned*)(vpn + d); o[d] += p * bf2f((u16)u); o[d + 1] += p * bf2f((u16)(u >> 16)); }
            }
        }
#pragma unroll
        for (int d = 0; d < BDH; d += 2) *(unsigned*)(lds + row * OST + (h * BDH + d) * 2) = (unsigned)f2bf(o[d]) | ((unsigned)f2bf(o[d + 1]) << 16);
    }
    __syncthreads();
    const u16* Wt = (const u16*)(a.ws + BWOT_OFF) + (size_t)(w * 160 + c16) * DM + 8 * g;
#pragma unroll 1
    for (int nh = 0; nh < 2; ++nh) {
        f32x4 acc[5][2];
#pragma unroll
        for (int nt = 0; nt < 5; ++nt) { acc[nt][0] = (f32x4){0.f, 0.f, 0.f, 0.f}; acc[nt][1] = (f32x4){0.f, 0.f, 0.f, 0.f}; }
#pragma unroll 4
        for (int ks = 0; ks < 20; ++ks) {
            const bf16x8 o0 = *(const bf16x8*)(lds + c16 * OST + ks * 64 + g * 16), o1 = *(const bf16x8*)(lds + (16 + c16) * OST + ks * 64 + g * 16);
#pragma unroll
            for (int nt = 0; nt < 5; ++nt) {
                const bf16x8 wf = *(const bf16x8*)(Wt + (size_t)(nh * 5 + nt) * 16 * DM + ks * 32);
                acc[nt][0] = mfma32(wf, o0, acc[nt][0]);
                acc[nt][1] = mfma32(wf, o1, acc[nt][1]);
            }
        }
#pragma unroll
        for (int nt = 0; nt < 5; ++nt)
#pragma unroll
            for (int mt = 0; mt < 2; ++mt) {
                const float v0 = acc[nt][mt][0], v1 = acc[nt][mt][1], v2 = acc[nt][mt][2], v3 = acc[nt][mt][3];
                v2u pk;
                pk.x = (unsigned)f2bf(v0) | ((unsigned)f2bf(v1) << 16);
                pk.y = (unsigned)f2bf(v2) | ((unsigned)f2bf(v3) << 16);
                *(v2u*)(a.out + (size_t)(rb32 * 32 + mt * 16 + c16) * (2 * DM) + DM + w * 160 + (nh * 5 + nt) * 16 + 4 * g) = pk;
            }
    }
    __syncthreads();
}

constexpr int NWORK = 192;
constexpr int T_KV = 0, T_TR = T_KV + 1024, T_Q = T_TR + 740, T_BQ = T_Q + 512, T_BF = T_BQ + 240, T_SEG = T_BF + 64, T_END = T_SEG + 64 * 18;

__device__ __forceinline__ void worker_role(const Args& a, unsigned char* lds) {
    const int wid = blockIdx.x - 64, tid = threadIdx.x, w = tid >> 6;
    gu32* tmo = (gu32*)(a.ws + TMO_OFF);
    u16* Q = (u16*)(a.ws + Q_OFF); u16* K = (u16*)(a.ws + K_OFF); u16* Vt = (u16*)(a.ws + VT_OFF); u16* Bq = (u16*)(a.ws + BQKV_OFF);
    for (int T = wid; T < T_END; T += NWORK) {
        if (T < T_TR) {
            const int rb = T >> 6, r = T & 63, hc = r & 31;
            if (r < 32) gemm_tile<0>(a.Ax, DM, a.Wk, HD, K, HD, DM, rb * 128, hc * 128, lds);
            else gemm_tile<1>(a.Ax, DM, a.Wv, HD, Vt, HD, DM, rb * 128, hc * 128, lds);
            signal_flag(flagp(a.ws, F_KV + rb * 32 + hc));
        } else if (T < T_Q) {
            const int t = T - T_TR;
            if (t < 640) transpose_tile(a.Wo, DM, (u16*)(a.ws + WOT_OFF), HD, (t / 10) * 64, (t % 10) * 64, lds, a.ws);
            else transpose_tile(a.Bwo, DM, (u16*)(a.ws + BWOT_OFF), DM, ((t - 640) / 10) * 64, ((t - 640) % 10) * 64, lds, a.ws);
            signal_flag(flagp(a.ws, F_TR));
        } else if (T < T_BQ) {
            const int t = T - T_Q, rb = t >> 5, hc = t & 31;
            gemm_tile<0>(a.Ax, DM, a.Wq, HD, Q, HD, DM, rb * 128, hc * 128, lds);
            signal_flag(flagp(a.ws, F_Q + rb * 32 + hc));
        } else if (T < T_BF) {
            const int t = T - T_BQ, rb = t / 15, nt = t - rb * 15;
            gemm_tile<0>(a.Bx, DM, a.Bw, BD3, Bq, BD3, DM, rb * 128, nt * 128, lds);
            signal_flag(flagp(a.ws, F_BQ + rb));
        } else if (T < T_SEG) {
            const int rb32 = T - T_BF, rb = rb32 >> 2;
            const int fi = tid == 0 ? F_BQ + rb : (tid == 1 ? F_BQ + (rb > 0 ? rb - 1 : 0) : F_TR);
            wait_flags(flagp(a.ws, fi), tid < 2 ? 15u : 740u, tid < 3, tmo);
            bfinal_task(a, rb32, lds);
        } else {
            const int t = T - T_SEG, s = t / 18, r = t - s * 18, rb = s >> 2;
            if (r < 8) {
                const int hh = r * 4 + (tid & 3), kind = (tid >> 2) & 3;
                const int fi = kind == 0 ? F_KV + rb * 32 + hh : (kind == 1 ? F_Q + rb * 32 + hh : F_KM + s * 32 + hh);
                wait_flags(flagp(a.ws, fi), kind == 1 ? 1u : 2u, tid < 12, tmo);
                attn_x(a, s, r * 4 + w);
                signal_flag(flagp(a.ws, F_ATT + s));
            } else {
                wait_flags(flagp(a.ws, tid == 0 ? F_ATT + s : F_TR), tid == 0 ? 8u : 740u, tid < 2, tmo);
                oproj_task(a, s, r - 8, lds);
            }
        }
    }
}

__global__ __launch_bounds__(256, 1) void fused_kernel(Args a) {
    extern __shared__ __attribute__((aligned(16))) unsigned char lds[];
    if (blockIdx.x < 64) chain_role(a, lds);
    else worker_role(a, lds);
}

extern "C" void kernel_launch(void* const* d_in, const int* in_sizes, int n_in,
                              void* d_out, int out_size, void* d_ws, size_t ws_size,
                              hipStream_t stream) {
    const u16* Ax = (const u16*)d_in[0];
    const u16* mem0 = (const u16*)d_in[1];
    const u16* Wq = (const u16*)d_in[2];
    const u16* Wk = (const u16*)d_in[3];
    const u16* Wv = (const u16*)d_in[4];
    const u16* Wo = (const u16*)d_in[5];
    const u16* Bx = (const u16*)d_in[7];
    const u16* Bw = (const u16*)d_in[8];
    const u16* Bwo = (const u16*)d_in[9];
    unsigned char* ws = (unsigned char*)d_ws;
    u16* out = (u16*)d_out;

    (void)hipMemsetAsync(ws, 0, XCHG_BYTES, stream);
    Args a{Ax, mem0, Wq, Wk, Wv, Wo, Bx, Bw, Bwo, out, ws};
    (void)hipFuncSetAttribute((const void*)fused_kernel, hipFuncAttributeMaxDynamicSharedMemorySize, CHAIN_LDS);
    fused_kernel<<<256, 256, CHAIN_LDS, stream>>>(a);
}
```

```cpp
#include <hip/hip_runtime.h>
#include <hip/hip_bf16.h>
#include <stdint.h>

#define GAS __attribute__((address_space(1)))
#define LAS __attribute__((address_space(3)))
typedef unsigned short u16;
typedef short bf16x8 __attribute__((ext_vector_type(8)));
typedef short bf16x4 __attribute__((ext_vector_type(4)));
typedef float f32x4 __attribute__((ext_vector_type(4)));
typedef unsigned v4u __attribute__((ext_vector_type(4)));
typedef unsigned v2u __attribute__((ext_vector_type(2)));
typedef GAS unsigned long long gu64;
typedef GAS unsigned gu32;
typedef LAS bf16x4 lds_bf16x4;
#define RLX_AGENT __ATOMIC_RELAXED, __HIP_MEMORY_SCOPE_AGENT

constexpr int SEQ = 2048, DM = 640, NH = 32, DH = 128, MEMN = 8, SEG = 32, NSEG = 64, HD = 4096;
constexpr int BD3 = 1920, BDH = 20;

constexpr size_t XA_OFF = 0;
constexpr size_t XA_BYTES = (size_t)2 * 64 * 512 * 8;
constexpr size_t XB_OFF = XA_OFF + XA_BYTES;
constexpr size_t XB_BYTES = (size_t)2 * 64 * 2560 * 8;
constexpr size_t XC_OFF = XB_OFF + XB_BYTES;
constexpr int XC_REP = 8;
constexpr size_t XC_BYTES = (size_t)2 * XC_REP * 2560 * 8;
constexpr size_t TMO_OFF = XC_OFF + XC_BYTES;
constexpr size_t FL_OFF = TMO_OFF + 256;
constexpr int F_KV = 0, F_Q = F_KV + 512, F_KM = F_Q + 512, F_ATT = F_KM + 2048, F_BQ = F_ATT + 64, F_TR = F_BQ + 16, F_END = F_TR + 1;
constexpr size_t XCHG_BYTES = (FL_OFF + (size_t)F_END * 64 + 255) / 256 * 256;
constexpr size_t Q_OFF = (XCHG_BYTES + 4095) / 4096 * 4096;
constexpr size_t K_OFF = Q_OFF + (size_t)SEQ * HD * 2;
constexpr size_t VT_OFF = K_OFF + (size_t)SEQ * HD * 2;
constexpr size_t KM_OFF = VT_OFF + (size_t)SEQ * HD * 2;
constexpr size_t VM_OFF = KM_OFF + (size_t)NSEG * MEMN * HD * 2;
constexpr size_t O_OFF = VM_OFF + (size_t)NSEG * MEMN * HD * 2;
constexpr size_t BQKV_OFF = O_OFF + (size_t)SEQ * HD * 2;
constexpr size_t WOT_OFF = BQKV_OFF + (size_t)SEQ * BD3 * 2;
constexpr size_t BWOT_OFF = WOT_OFF + (size_t)DM * HD * 2;
constexpr size_t WS_END = BWOT_OFF + (size_t)DM * DM * 2;

__device__ __forceinline__ int opaque_tid() { int t = threadIdx.x; asm volatile("" : "+v"(t)); return t; }
__device__ __forceinline__ u16 f2bf(float f) { return __builtin_bit_cast(u16, (__bf16)f); }
__device__ __forceinline__ float bf2f(u16 b) { return __builtin_bit_cast(float, (unsigned)b << 16); }
__device__ __forceinline__ float rbf(float f) { return bf2f(f2bf(f)); }
__device__ __forceinline__ f32x4 mfma32(bf16x8 a, bf16x8 b, f32x4 c) { return __builtin_amdgcn_mfma_f32_16x16x32_bf16(a, b, c, 0, 0, 0); }

__device__ __forceinline__ void st8(u16* p, v2u v) { __hip_atomic_store((gu64*)p, (unsigned long long)v.x | ((unsigned long long)v.y << 32), RLX_AGENT); }
__device__ __forceinline__ unsigned off_b(unsigned row, unsigned ch) { return 256u * row + 16u * (ch ^ (((row & 3) << 2) | ((row >> 2) & 3))); }

template <int MODE>
__device__ __forceinline__ void gemm_tile(const u16* __restrict__ A, int lda, const u16* __restrict__ W, int ldw,
                                          u16* __restrict__ C, int ldc, int K, int m0, int n0, unsigned char* lds) {
    const int tid = opaque_tid(), w = tid >> 6, lane = tid & 63, c16 = lane & 15, g = lane >> 4;
    const int wm = w >> 1, wn = w & 1;
    unsigned char* LA = lds;
    unsigned char* LW = lds + 32768;
    f32x4 acc[4][4];
#pragma unroll
    for (int i = 0; i < 4; ++i)
#pragma unroll
        for (int jn = 0; jn < 4; ++jn) acc[i][jn] = (f32x4){0.f, 0.f, 0.f, 0.f};
    v4u ra[4], rw[4];
    auto gload = [&](int kt) {
#pragma unroll
        for (int i = 0; i < 4; ++i) {
            const int c = tid + 256 * i;
            ra[i] = *(const v4u*)(A + (size_t)(m0 + (c >> 3)) * lda + kt * 64 + (c & 7) * 8);
            rw[i] = *(const v4u*)(W + (size_t)(kt * 64 + (c >> 4)) * ldw + n0 + (c & 15) * 8);
        }
    };
    auto lstore = [&](int buf) {
#pragma unroll
        for (int i = 0; i < 4; ++i) {
            const int c = tid + 256 * i;
            const int row = c >> 3, ch = c & 7;
            *(v4u*)(LA + buf * 16384 + row * 128 + 16 * (ch ^ (row & 7))) = ra[i];
            const int r2 = c >> 4, ch2 = c & 15;
            *(v4u*)(LW + buf * 16384 + (r2 >> 5) * 8192 + off_b(r2 & 31, ch2)) = rw[i];
        }
    };
    const int nk = K / 64;
    gload(0); lstore(0); __syncthreads();
    const unsigned q4 = (lane & 15) >> 2, p4 = lane & 3;
#pragma unroll 1
    for (int kt = 0; kt < nk; ++kt) {
        const int buf = kt & 1;
        if (kt + 1 < nk) gload(kt + 1);
#pragma unroll
        for (int ks = 0; ks < 2; ++ks) {
            bf16x8 af[4], wf[4];
#pragma unroll
            for (int mt = 0; mt < 4; ++mt) {
                const int row = wm * 64 + mt * 16 + c16, ch = ks * 4 + g;
                af[mt] = *(const bf16x8*)(LA + buf * 16384 + row * 128 + 16 * (ch ^ (row & 7)));
            }
#pragma unroll
            for (int nt = 0; nt < 4; ++nt) {
                const unsigned cb = wn * 4 + nt;
                unsigned char* base = LW + buf * 16384 + ks * 8192;
                const bf16x4 lo = __builtin_amdgcn_ds_read_tr16_b64_v4i16((lds_bf16x4*)(base + off_b(8 * g + q4, 2 * cb + (p4 >> 1)) + 8 * (p4 & 1)));
                const bf16x4 hi = __builtin_amdgcn_ds_read_tr16_b64_v4i16((lds_bf16x4*)(base + off_b(8 * g + 4 + q4, 2 * cb + (p4 >> 1)) + 8 * (p4 & 1)));
                wf[nt] = __builtin_shufflevector(lo, hi, 0, 1, 2, 3, 4, 5, 6, 7);
            }
#pragma unroll
            for (int mt = 0; mt < 4; ++mt)
#pragma unroll
                for (int nt = 0; nt < 4; ++nt)
                    acc[mt][nt] = (MODE == 0) ? mfma32(wf[nt], af[mt], acc[mt][nt]) : mfma32(af[mt], wf[nt], acc[mt][nt]);
        }
        if (kt + 1 < nk) lstore(buf ^ 1);
        __syncthreads();
    }
#pragma unroll
    for (int mt = 0; mt < 4; ++mt)
#pragma unroll
        for (int nt = 0; nt < 4; ++nt) {
            v2u pk;
            pk.x = (unsigned)f2bf(acc[mt][nt][0]) | ((unsigned)f2bf(acc[mt][nt][1]) << 16);
            pk.y = (unsigned)f2bf(acc[mt][nt][2]) | ((unsigned)f2bf(acc[mt][nt][3]) << 16);
            if (MODE == 0) {
                const int m = m0 + wm * 64 + mt * 16 + c16, n = n0 + wn * 64 + nt * 16 + 4 * g;
                st8(C + (size_t)m * ldc + n, pk);
            } else {
                const int m = m0 + wm * 64 + mt * 16 + 4 * g, n = n0 + wn * 64 + nt * 16 + c16;
                st8(C + ((size_t)(m >> 5) * HD + n) * 32 + 8 * g + 4 * ((m >> 4) & 1), pk);
            }
        }
}

struct Args { const u16* Ax; const u16* mem0; const u16* Wq; const u16* Wk; const u16* Wv; const u16* Wo;
              const u16* Bx; const u16* Bw; const u16* Bwo; u16* out; unsigned char* ws; };

constexpr unsigned kSpinCap = 1u << 17;

typedef __amdgpu_buffer_rsrc_t rsrc_t;
__device__ __forceinline__ void st16(rsrc_t rs, int off, v4u v) { __builtin_amdgcn_raw_buffer_store_b128(v, rs, off, 0, 16); }
template <int NPT, bool TAG16>
__device__ __forceinline__ void gather(rsrc_t rs, int off, int str, unsigned tag, v4u (&v)[NPT], gu32* tmo) {
    for (unsigned spins = 0;; ++spins) {
        asm volatile("" ::: "memory");
#pragma unroll
        for (int k = 0; k < NPT; ++k) v[k] = __builtin_amdgcn_raw_buffer_load_b128(rs, off + k * str, 0, 16);
        bool ok = true;
#pragma unroll
        for (int k = 0; k < NPT; ++k) {
            if (TAG16) ok &= ((v[k].y >> 16) == tag) & ((v[k].w >> 16) == tag);
            else ok &= (v[k].y == tag) & (v[k].w == tag);
        }
        if (__all(ok)) break;
        if ((spins & 63u) == 63u) {
            if (spins > kSpinCap) { __hip_atomic_store(tmo, 1u, RLX_AGENT); break; }
            if (__hip_atomic_load(tmo, RLX_AGENT) != 0u) break;
        }
    }
}
__device__ __forceinline__ float xmax16_32(float v) {
    auto r = __builtin_amdgcn_permlane16_swap(__float_as_uint(v), __float_as_uint(v), false, false);
    v = fmaxf(__uint_as_float(r[0]), __uint_as_float(r[1]));
    auto r2 = __builtin_amdgcn_permlane32_swap(__float_as_uint(v), __float_as_uint(v), false, false);
    return fmaxf(__uint_as_float(r2[0]), __uint_as_float(r2[1]));
}
__device__ __forceinline__ float xsum16_32(float v) {
    auto r = __builtin_amdgcn_permlane16_swap(__float_as_uint(v), __float_as_uint(v), false, false);
    v = __uint_as_float(r[0]) + __uint_as_float(r[1]);
    auto r2 = __builtin_amdgcn_permlane32_swap(__float_as_uint(v), __float_as_uint(v), false, false);
    return __uint_as_float(r2[0]) + __uint_as_float(r2[1]);
}

constexpr int MEM_STRIDE = 1296, QK_STRIDE = 272;
constexpr int L_WO = 0;
constexpr int L_MEM = L_WO + 81920, L_Q = L_MEM + 8 * MEM_STRIDE, L_K = L_Q + 8 * QK_STRIDE, L_VT = L_K + 8 * QK_STRIDE,
              L_RED = L_VT + 128 * 32, L_END = L_RED + 6 * 80 * 4;
constexpr int L_STAGE = L_MEM;
constexpr int CHAIN_LDS = (L_STAGE + 51200 > L_END) ? (L_STAGE + 51200) : L_END;

__device__ __forceinline__ gu32* flagp(unsigned char* ws, int idx) { return (gu32*)(ws + FL_OFF + (size_t)idx * 64); }

__device__ __forceinline__ void chain_role(const Args& a, unsigned char* lds) {
    const int cu = blockIdx.x, h = cu >> 1, j = cu & 1;
    const int tid = threadIdx.x, w = tid >> 6, lane = tid & 63, c16 = lane & 15, g = lane >> 4;
    gu32* tmo = (gu32*)(a.ws + TMO_OFF);
    const rsrc_t xr = __builtin_amdgcn_make_buffer_rsrc((void*)a.ws, 0, (int)TMO_OFF, 0x00020000);
    const rsrc_t kr_ = __builtin_amdgcn_make_buffer_rsrc((void*)(a.ws + K_OFF), 0, SEQ * HD * 2, 0x00020000);
    const rsrc_t vr_ = __builtin_amdgcn_make_buffer_rsrc((void*)(a.ws + VT_OFF), 0, SEQ * HD * 2, 0x00020000);
    u16* Km = (u16*)(a.ws + KM_OFF);
    u16* Vm = (u16*)(a.ws + VM_OFF);

    bf16x8 wqkv[3][20];
    {
        unsigned char* stg = lds + L_STAGE;
        const u16* Wqk = j ? a.Wk : a.Wq;
        const unsigned q4 = (lane & 15) >> 2, p4 = lane & 3;
#pragma unroll
        for (int rd = 0; rd < 5; ++rd) {
            v4u tmp[12];
#pragma unroll
            for (int i = 0; i < 12; ++i) {
                const int c = tid + 256 * i;
                const int row = c / 24, ch = c - row * 24;
                const size_t kr = (size_t)(rd * 128 + row) * HD + h * DH;
                tmp[i] = (ch < 16) ? *(const v4u*)(Wqk + kr + ch * 8) : *(const v4u*)(a.Wv + kr + j * 64 + (ch - 16) * 8);
            }
#pragma unroll
            for (int i = 0; i < 12; ++i) {
                const int c = tid + 256 * i;
                const int row = c / 24, ch = c - row * 24;
                *(v4u*)(stg + row * 400 + ch * 16) = tmp[i];
            }
            __syncthreads();
#pragma unroll
            for (int k4 = 0; k4 < 4; ++k4)
#pragma unroll
                for (int t = 0; t < 3; ++t) {
                    const int col = (t < 2) ? ((2 * w + t) * 16) : (128 + w * 16);
                    unsigned char* p0 = stg + (k4 * 32 + 8 * g + q4) * 400 + (col + 4 * p4) * 2;
                    const bf16x4 lo = __builtin_amdgcn_ds_read_tr16_b64_v4i16((lds_bf16x4*)p0);
                    const bf16x4 hi = __builtin_amdgcn_ds_read_tr16_b64_v4i16((lds_bf16x4*)(p0 + 4 * 400));
                    wqkv[t][rd * 4 + k4] = __builtin_shufflevector(lo, hi, 0, 1, 2, 3, 4, 5, 6, 7);
                }
            __syncthreads();
        }
#pragma unroll
        for (int rd = 0; rd < 2; ++rd) {
            v4u tmp[10];
#pragma unroll
            for (int i = 0; i < 10; ++i) {
                const int c = tid + 256 * i;
                const int row = c / 40, ch = c - row * 40;
                tmp[i] = *(const v4u*)(a.Wo + (size_t)(h * DH + rd * 64 + row) * DM + j * 320 + ch * 8);
            }
#pragma unroll
            for (int i = 0; i < 10; ++i) {
                const int c = tid + 256 * i;
                const int row = c / 40, ch = c - row * 40;
                *(v4u*)(stg + row * 656 + ch * 16) = tmp[i];
            }
            __syncthreads();
#pragma unroll
            for (int k2 = 0; k2 < 2; ++k2)
#pragma unroll
                for (int nt = 0; nt < 5; ++nt) {
                    unsigned char* p0 = stg + (k2 * 32 + 4 * g + q4) * 656 + (w * 80 + nt * 16 + 4 * p4) * 2;
                    const bf16x4 lo = __builtin_amdgcn_ds_read_tr16_b64_v4i16((lds_bf16x4*)p0);
                    const bf16x4 hi = __builtin_amdgcn_ds_read_tr16_b64_v4i16((lds_bf16x4*)(p0 + 16 * 656));
                    *(bf16x8*)(lds + L_WO + ((w * 5 + nt) * 4 + rd * 2 + k2) * 1024 + lane * 16) = __builtin_shufflevector(lo, hi, 0, 1, 2, 3, 4, 5, 6, 7);
                }
            __syncthreads();
        }
        if (tid < 128) *(v4u*)(lds + L_VT + tid * 32 + 16) = (v4u){0u, 0u, 0u, 0u};
    }
    const float scale = 0.08838834764831845f;
    const int tt = tid < 240 ? tid : 239, hg = tt / 40, pr = tt - hg * 40;

    for (int s = 0; s < NSEG; ++s) {
        const unsigned tag = (unsigned)(s + 1);
        const int par = s & 1;
        if ((s & 3) == 0) {
            gu32* fl = flagp(a.ws, F_KV + (s >> 2) * 32 + h);
            for (unsigned spins = 0;; ++spins) {
                if (__builtin_amdgcn_readfirstlane(__hip_atomic_load(fl, RLX_AGENT)) >= 2u) break;
                __builtin_amdgcn_s_sleep(4);
                if ((spins & 63u) == 63u) {
                    if (spins > kSpinCap) { __hip_atomic_store(tmo, 1u, RLX_AGENT); break; }
                    if (__hip_atomic_load(tmo, RLX_AGENT) != 0u) break;
                }
            }
            __builtin_amdgcn_fence(__ATOMIC_ACQUIRE, "wavefront");
        }
        bf16x8 kx[2][4], vx[8];
#pragma unroll
        for (int kt = 0; kt < 2; ++kt)
#pragma unroll
            for (int ks = 0; ks < 4; ++ks)
                kx[kt][ks] = __builtin_bit_cast(bf16x8, __builtin_amdgcn_raw_buffer_load_b128(kr_, ((s * SEG + kt * 16 + c16) * HD + h * DH + 32 * ks + 8 * g) * 2, 0, 16));
#pragma unroll
        for (int et = 0; et < 8; ++et)
            vx[et] = __builtin_bit_cast(bf16x8, __builtin_amdgcn_raw_buffer_load_b128(vr_, ((s * HD + h * DH + et * 16 + c16) * 32 + 8 * g) * 2, 0, 16));
        if (s == 0) {
            for (int i = tid; i < 8 * 80; i += 256) {
                const int tk = i / 80, ch = i % 80;
                *(v4u*)(lds + L_MEM + tk * MEM_STRIDE + ch * 16) = *(const v4u*)(a.mem0 + tk * DM + ch * 8);
            }
        } else {
            v4u v[5];
            __builtin_amdgcn_s_sleep(16);
            gather<5, false>(xr, (int)XC_OFF + (((s - 1) & 1) * XC_REP + (cu & (XC_REP - 1))) * 20480 + tid * 16, 4096, (unsigned)s, v, tmo);
            {
                const int l2 = (2 * tid) & 63;
                const int tok = l2 & 7, col = ((tid >> 5) * 16 + (l2 >> 4) * 4 + ((l2 >> 3) & 1) * 2) * 2;
#pragma unroll
                for (int k = 0; k < 5; ++k) {
                    *(unsigned*)(lds + L_MEM + tok * MEM_STRIDE + col + k * 256) = v[k].x;
                    *(unsigned*)(lds + L_MEM + (tok + 1) * MEM_STRIDE + col + k * 256) = v[k].z;
                }
            }
        }
        __syncthreads();
        f32x4 acc[3];
        {
            bf16x8 mb[20];
#pragma unroll
            for (int ks = 0; ks < 20; ++ks) mb[ks] = *(const bf16x8*)(lds + L_MEM + (c16 & 7) * MEM_STRIDE + ks * 64 + g * 16);
            __builtin_amdgcn_sched_barrier(0);
#pragma unroll
            for (int t = 0; t < 3; ++t) acc[t] = (f32x4){0.f, 0.f, 0.f, 0.f};
#pragma unroll
            for (int ks = 0; ks < 20; ++ks)
#pragma unroll
                for (int t = 0; t < 3; ++t) acc[t] = mfma32(wqkv[t][ks], mb[ks], acc[t]);
        }
        u16 qv[3][4];
#pragma unroll
        for (int t = 0; t < 3; ++t)
#pragma unroll
            for (int r = 0; r < 4; ++r) { const float f = acc[t][r]; qv[t][r] = f2bf(f); }
        if (s + 1 < NSEG) {
            const bool hi = c16 >= 8;
            v4u pk;
            pk.x = (unsigned)(hi ? qv[0][2] : qv[0][0]) | ((unsigned)(hi ? qv[1][2] : qv[1][0]) << 16);
            pk.y = (unsigned)(hi ? qv[2][2] : qv[2][0]) | (tag << 16);
            pk.z = (unsigned)(hi ? qv[0][3] : qv[0][1]) | ((unsigned)(hi ? qv[1][3] : qv[1][1]) << 16);
            pk.w = (unsigned)(hi ? qv[2][3] : qv[2][1]) | (tag << 16);
            st16(xr, (int)XA_OFF + (par * 64 + cu) * 4096 + tid * 16, pk);
        }
        if (c16 < 8) {
            const int token = c16;
            unsigned char* qk = lds + (j ? L_K : L_Q) + token * QK_STRIDE;
#pragma unroll
            for (int t = 0; t < 2; ++t) {
                v2u pk; pk.x = (unsigned)qv[t][0] | ((unsigned)qv[t][1] << 16); pk.y = (unsigned)qv[t][2] | ((unsigned)qv[t][3] << 16);
                *(v2u*)(qk + ((2 * w + t) * 16 + 4 * g) * 2) = pk;
                if (j) st8(Km + (size_t)(s * MEMN + token) * HD + h * DH + (2 * w + t) * 16 + 4 * g, pk);
            }
            {
                v2u pk; pk.x = (unsigned)qv[2][0] | ((unsigned)qv[2][1] << 16); pk.y = (unsigned)qv[2][2] | ((unsigned)qv[2][3] << 16);
                st8(Vm + (size_t)(s * MEMN + token) * HD + h * DH + j * 64 + w * 16 + 4 * g, pk);
            }
#pragma unroll
            for (int r = 0; r < 4; ++r) {
                const int e = j * 64 + w * 16 + 4 * g + r;
                *(u16*)(lds + L_VT + e * 32 + token * 2) = qv[2][r];
            }
        }
        if (s + 1 == NSEG) {
            asm volatile("s_waitcnt vmcnt(0)" ::: "memory");
            __syncthreads();
            if (tid == 0) __hip_atomic_fetch_add(flagp(a.ws, F_KM + s * 32 + h), 1u, RLX_AGENT);
        }
        if (s + 1 == NSEG) break;
        {
            v4u v[1];
            gather<1, true>(xr, (int)XA_OFF + (par * 64 + (cu ^ 1)) * 4096 + tid * 16, 0, tag, v, tmo);
            const int pj = j ^ 1;
            const int pw = tid >> 6, pg = (tid >> 4) & 3, token = tid & 7, r = ((tid >> 3) & 1) * 2;
            unsigned char* qk = lds + (pj ? L_K : L_Q) + token * QK_STRIDE;
            *(unsigned*)(qk + ((2 * pw) * 16 + 4 * pg + r) * 2) = (v[0].x & 0xffffu) | (v[0].z << 16);
            *(unsigned*)(qk + ((2 * pw + 1) * 16 + 4 * pg + r) * 2) = (v[0].x >> 16) | (v[0].z & 0xffff0000u);
            *(u16*)(lds + L_VT + (pj * 64 + pw * 16 + 4 * pg + r) * 32 + token * 2) = (u16)v[0].y;
            *(u16*)(lds + L_VT + (pj * 64 + pw * 16 + 4 * pg + r + 1) * 32 + token * 2) = (u16)v[0].w;
        }
        asm volatile("s_waitcnt vmcnt(0)" ::: "memory");
        __syncthreads();
        if (tid == 0) __hip_atomic_fetch_add(flagp(a.ws, F_KM + s * 32 + h), 1u, RLX_AGENT);
        bf16x8 qf[4], kf[4];
        bf16x4 vm4[8];
#pragma unroll
        for (int ks = 0; ks < 4; ++ks) {
            qf[ks] = *(const bf16x8*)(lds + L_Q + (c16 & 7) * QK_STRIDE + ks * 64 + g * 16);
            kf[ks] = *(const bf16x8*)(lds + L_K + (c16 & 7) * QK_STRIDE + ks * 64 + g * 16);
        }
#pragma unroll
        for (int et = 0; et < 8; ++et) vm4[et] = *(const bf16x4*)(lds + L_VT + (et * 16 + c16) * 32 + g * 8);
        __builtin_amdgcn_sched_barrier(0);
        f32x4 st[3];
#pragma unroll
        for (int t = 0; t < 3; ++t) st[t] = (f32x4){0.f, 0.f, 0.f, 0.f};
#pragma unroll
        for (int ks = 0; ks < 4; ++ks) {
            st[1] = mfma32(kx[0][ks], qf[ks], st[1]);
            st[2] = mfma32(kx[1][ks], qf[ks], st[2]);
            st[0] = mfma32(kf[ks], qf[ks], st[0]);
        }
        __builtin_amdgcn_sched_barrier(0);
        bf16x8 wo[5][4];
#pragma unroll
        for (int nt = 0; nt < 5; ++nt)
#pragma unroll
            for (int ks = 0; ks < 4; ++ks) wo[nt][ks] = *(const bf16x8*)(lds + L_WO + ((w * 5 + nt) * 4 + ks) * 1024 + lane * 16);
        __builtin_amdgcn_sched_barrier(0);
        float x[3][4];
        float mx = -INFINITY;
#pragma unroll
        for (int t = 0; t < 3; ++t)
#pragma unroll
            for (int r = 0; r < 4; ++r) {
                const float sv = st[t][r];
                float v = rbf(rbf(sv) * scale);
                if (t == 0 && g >= 2) v = -INFINITY;
                x[t][r] = v; mx = fmaxf(mx, v);
            }
        mx = xmax16_32(mx);
        float sum = 0.f;
#pragma unroll
        for (int t = 0; t < 3; ++t)
#pragma unroll
            for (int r = 0; r < 4; ++r) { x[t][r] = __expf(x[t][r] - mx); sum += x[t][r]; }
        sum = xsum16_32(sum);
        const float inv = 1.0f / sum;
        bf16x8 px, pm;
#pragma unroll
        for (int r = 0; r < 4; ++r) {
            pm[r] = (short)f2bf(x[0][r] * inv); pm[4 + r] = 0;
            px[r] = (short)f2bf(x[1][r] * inv);
            px[4 + r] = (short)f2bf(x[2][r] * inv);
        }
        f32x4 oa[8];
#pragma unroll
        for (int et = 0; et < 8; ++et) oa[et] = mfma32(vx[et], px, (f32x4){0.f, 0.f, 0.f, 0.f});
#pragma unroll
        for (int et = 0; et < 8; ++et) {
            const bf16x8 vm = __builtin_shufflevector(vm4[et], (bf16x4){0, 0, 0, 0}, 0, 1, 2, 3, 4, 5, 6, 7);
            oa[et] = mfma32(vm, pm, oa[et]);
        }
        bf16x8 of[4];
#pragma unroll
        for (int et = 0; et < 8; ++et)
#pragma unroll
            for (int r = 0; r < 4; ++r) { const float f = oa[et][r]; of[et >> 1][(et & 1) * 4 + r] = (short)f2bf(f); }
        {
            f32x4 p[5];
#pragma unroll
            for (int nt = 0; nt < 5; ++nt) p[nt] = (f32x4){0.f, 0.f, 0.f, 0.f};
#pragma unroll
            for (int ks = 0; ks < 4; ++ks)
#pragma unroll
                for (int nt = 0; nt < 5; ++nt) p[nt] = mfma32(wo[nt][ks], of[ks], p[nt]);
            const int off = (int)XB_OFF + (par * 32 * 2560 + h * 2560 + (j * 4 + w) * 320 + lane) * 16;
            const bool hi = c16 >= 8;
#pragma unroll
            for (int nt = 0; nt < 5; ++nt) {
                const float p0 = p[nt][0], p1 = p[nt][1], p2 = p[nt][2], p3 = p[nt][3];
                v4u pk; pk.x = __float_as_uint(hi ? p2 : p0); pk.y = tag; pk.z = __float_as_uint(hi ? p3 : p1); pk.w = tag;
                st16(xr, off + nt * 1024, pk);
            }
        }
        {
            const int off = (int)XB_OFF + (par * 32 * 2560 + hg * 2560 + cu * 40 + pr) * 16;
            const int off5 = off + (hg < 2 ? 5 : 4) * 6 * 2560 * 16;
            v4u v[6];
            __builtin_amdgcn_s_sleep(16);
            for (unsigned spins = 0;; ++spins) {
                asm volatile("" ::: "memory");
#pragma unroll
                for (int k = 0; k < 5; ++k) v[k] = __builtin_amdgcn_raw_buffer_load_b128(xr, off + k * 6 * 2560 * 16, 0, 16);
                v[5] = __builtin_amdgcn_raw_buffer_load_b128(xr, off5, 0, 16);
                bool ok = true;
#pragma unroll
                for (int k = 0; k < 6; ++k) ok &= (v[k].y == tag) & (v[k].w == tag);
                if (__all(ok)) break;
                if ((spins & 63u) == 63u) {
                    if (spins > kSpinCap) { __hip_atomic_store(tmo, 1u, RLX_AGENT); break; }
                    if (__hip_atomic_load(tmo, RLX_AGENT) != 0u) break;
                }
            }
            float s0 = 0.f, s1 = 0.f;
#pragma unroll
            for (int k = 0; k < 5; ++k) { s0 += __uint_as_float(v[k].x); s1 += __uint_as_float(v[k].z); }
            if (hg < 2) { s0 += __uint_as_float(v[5].x); s1 += __uint_as_float(v[5].z); }
            if (tid < 240) { v2u pk; pk.x = __float_as_uint(s0); pk.y = __float_as_uint(s1); *(v2u*)(lds + L_RED + tid * 8) = pk; }
        }
        __syncthreads();
        if (tid < 128) {
            const int t80 = tid < 80 ? tid : 79;
            const float* rp = (const float*)(lds + L_RED) + t80;
            const float sacc = ((rp[0] + rp[80]) + (rp[160] + rp[240])) + (rp[320] + rp[400]);
            const unsigned mine = f2bf(sacc);
            const unsigned m1 = (unsigned)__builtin_amdgcn_mov_dpp((int)mine, 0x55, 0xf, 0xf, true);
            const unsigned m2 = (unsigned)__builtin_amdgcn_mov_dpp((int)mine, 0xAA, 0xf, 0xf, true);
            const unsigned m3 = (unsigned)__builtin_amdgcn_mov_dpp((int)mine, 0xFF, 0xf, 0xf, true);
            if (tid < 80 && (tid & 3) == 0) {
                v4u pk; pk.x = mine | (m1 << 16); pk.y = tag; pk.z = m2 | (m3 << 16); pk.w = tag;
#pragma unroll
                for (int rp = 0; rp < XC_REP; ++rp) st16(xr, (int)XC_OFF + (par * XC_REP + rp) * 20480 + (cu * 20 + (tid >> 2)) * 16, pk);
            }
        }
    }
}

__device__ __forceinline__ void wait_flags(gu32* myflag, unsigned target, bool active, gu32* tmo) {
    if (threadIdx.x < 64) {
        for (unsigned spins = 0;; ++spins) {
            const bool ok = !active || (__hip_atomic_load(myflag, RLX_AGENT) >= target);
            if (__all(ok)) break;
            __builtin_amdgcn_s_sleep(8);
            if ((spins & 63u) == 63u) {
                if (spins > kSpinCap) { __hip_atomic_store(tmo, 1u, RLX_AGENT); break; }
                if (__hip_atomic_load(tmo, RLX_AGENT) != 0u) break;
            }
        }
        __builtin_amdgcn_fence(__ATOMIC_ACQUIRE, "agent");
        asm volatile("s_waitcnt vmcnt(0)" ::: "memory");
    }
    __syncthreads();
}
__device__ __forceinline__ void signal_flag(gu32* flag) {
    asm volatile("s_waitcnt vmcnt(0)" ::: "memory");
    __syncthreads();
    if (threadIdx.x == 0) __hip_atomic_fetch_add(flag, 1u, RLX_AGENT);
}

__device__ __forceinline__ void transpose_tile(const u16* __restrict__ S, int lds_, u16* D, int ldd, int k0, int n0, unsigned char* lds, unsigned char* ws) {
    const int tid = opaque_tid();
    u16* T = (u16*)lds;
#pragma unroll
    for (int i = 0; i < 2; ++i) {
        const int c = tid + 256 * i, row = c >> 3, ch = c & 7;
        const v4u v = *(const v4u*)(S + (size_t)(k0 + row) * lds_ + n0 + ch * 8);
        const unsigned wv[4] = {v.x, v.y, v.z, v.w};
#pragma unroll
        for (int e = 0; e < 4; ++e) {
            T[(ch * 8 + 2 * e) * 72 + row] = (u16)wv[e];
            T[(ch * 8 + 2 * e + 1) * 72 + row] = (u16)(wv[e] >> 16);
        }
    }
    __syncthreads();
    const rsrc_t dr = __builtin_amdgcn_make_buffer_rsrc((void*)D, 0, DM * ldd * 2, 0x00020000);
#pragma unroll
    for (int i = 0; i < 2; ++i) {
        const int c = tid + 256 * i, n = c >> 3, ch = c & 7;
        const v4u v = *(const v4u*)(T + n * 72 + ch * 8);
        st16(dr, ((n0 + n) * ldd + k0 + ch * 8) * 2, v);
    }
}

__device__ __forceinline__ void attn_x(const Args& a, int s, int h) {
    const int lane = opaque_tid() & 63, c16 = lane & 15, g = lane >> 4;
    const u16* Q = (const u16*)(a.ws + Q_OFF); const u16* K = (const u16*)(a.ws + K_OFF); const u16* Vt = (const u16*)(a.ws + VT_OFF);
    const u16* Km = (const u16*)(a.ws + KM_OFF); const u16* Vm = (const u16*)(a.ws + VM_OFF); u16* O = (u16*)(a.ws + O_OFF);
    const float scale = 0.08838834764831845f;
    bf16x8 qf[4][2], kf[4][3], vxf[8], vmf[8];
#pragma unroll
    for (int ks = 0; ks < 4; ++ks) {
        const int eo = h * DH + 32 * ks + 8 * g;
        qf[ks][0] = *(const bf16x8*)(Q + (size_t)(s * SEG + c16) * HD + eo);
        qf[ks][1] = *(const bf16x8*)(Q + (size_t)(s * SEG + 16 + c16) * HD + eo);
        kf[ks][0] = *(const bf16x8*)(Km + (size_t)(s * MEMN + (c16 & 7)) * HD + eo);
        kf[ks][1] = *(const bf16x8*)(K + (size_t)(s * SEG + c16) * HD + eo);
        kf[ks][2] = *(const bf16x8*)(K + (size_t)(s * SEG + 16 + c16) * HD + eo);
    }
#pragma unroll
    for (int et = 0; et < 8; ++et) {
        const int e = h * DH + et * 16 + c16;
        vxf[et] = *(const bf16x8*)(Vt + ((size_t)s * HD + e) * 32 + 8 * g);
#pragma unroll
        for (int r = 0; r < 4; ++r) { vmf[et][r] = (short)Vm[(size_t)(s * MEMN + 4 * (g & 1) + r) * HD + e]; vmf[et][4 + r] = 0; }
    }
    __builtin_amdgcn_sched_barrier(0);
    f32x4 st[3][2];
#pragma unroll
    for (int t = 0; t < 3; ++t) { st[t][0] = (f32x4){0.f, 0.f, 0.f, 0.f}; st[t][1] = (f32x4){0.f, 0.f, 0.f, 0.f}; }
#pragma unroll
    for (int ks = 0; ks < 4; ++ks)
#pragma unroll
        for (int t = 0; t < 3; ++t) { st[t][0] = mfma32(kf[ks][t], qf[ks][0], st[t][0]); st[t][1] = mfma32(kf[ks][t], qf[ks][1], st[t][1]); }
    bf16x8 px[2], pm[2];
#pragma unroll
    for (int qt = 0; qt < 2; ++qt) {
        const int qi = qt * 16 + c16;
        float x[3][4]; float mx = -INFINITY;
#pragma unroll
        for (int t = 0; t < 3; ++t)
#pragma unroll
            for (int r = 0; r < 4; ++r) {
                const float sv = st[t][qt][r];
                float v = rbf(rbf(sv) * scale);
                const int key = (t - 1) * 16 + 4 * g + r;
                const bool valid = (t == 0) ? (g < 2) : (key <= qi);
                if (!valid) v = -INFINITY;
                x[t][r] = v; mx = fmaxf(mx, v);
            }
        mx = xmax16_32(mx);
        float sum = 0.f;
#pragma unroll
        for (int t = 0; t < 3; ++t)
#pragma unroll
            for (int r = 0; r < 4; ++r) { x[t][r] = __expf(x[t][r] - mx); sum += x[t][r]; }
        sum = xsum16_32(sum);
        const float inv = 1.0f / sum;
#pragma unroll
        for (int r = 0; r < 4; ++r) {
            pm[qt][r] = (short)f2bf(x[0][r] * inv); pm[qt][4 + r] = 0;
            px[qt][r] = (short)f2bf(x[1][r] * inv);
            px[qt][4 + r] = (short)f2bf(x[2][r] * inv);
        }
    }
#pragma unroll
    for (int et = 0; et < 8; ++et) {
        const bf16x8 vx = vxf[et], vm = vmf[et];
#pragma unroll
        for (int qt = 0; qt < 2; ++qt) {
            f32x4 o = mfma32(vx, px[qt], (f32x4){0.f, 0.f, 0.f, 0.f});
            o = mfma32(vm, pm[qt], o);
            const float o0 = o[0], o1 = o[1], o2 = o[2], o3 = o[3];
            v2u pk;
            pk.x = (unsigned)f2bf(o0) | ((unsigned)f2bf(o1) << 16);
            pk.y = (unsigned)f2bf(o2) | ((unsigned)f2bf(o3) << 16);
            st8(O + (size_t)(s * SEG + qt * 16 + c16) * HD + h * DH + et * 16 + 4 * g, pk);
        }
    }
}

__device__ __forceinline__ void oproj_task(const Args& a, int s, int ns, unsigned char* lds) {
    const int tid = opaque_tid(), w = tid >> 6, lane = tid & 63, c16 = lane & 15, g = lane >> 4;
    const u16* O = (const u16*)(a.ws + O_OFF) + (size_t)(s * SEG + c16) * HD + w * 1024 + 8 * g;
    const u16* Wt = (const u16*)(a.ws + WOT_OFF) + (size_t)(ns * 64 + c16) * HD + w * 1024 + 8 * g;
    f32x4 acc[4][2];
#pragma unroll
    for (int nt = 0; nt < 4; ++nt) { acc[nt][0] = (f32x4){0.f, 0.f, 0.f, 0.f}; acc[nt][1] = (f32x4){0.f, 0.f, 0.f, 0.f}; }
#pragma unroll 1
    for (int kb = 0; kb < 4; ++kb) {
        bf16x8 of[8][2], wf[8][4];
#pragma unroll
        for (int k8 = 0; k8 < 8; ++k8) {
            const int ko = (kb * 8 + k8) * 32;
            of[k8][0] = *(const bf16x8*)(O + ko); of[k8][1] = *(const bf16x8*)(O + (size_t)16 * HD + ko);
#pragma unroll
            for (int nt = 0; nt < 4; ++nt) wf[k8][nt] = *(const bf16x8*)(Wt + (size_t)nt * 16 * HD + ko);
        }
        __builtin_amdgcn_sched_barrier(0);
#pragma unroll
        for (int k8 = 0; k8 < 8; ++k8)
#pragma unroll
            for (int nt = 0; nt < 4; ++nt) {
                acc[nt][0] = mfma32(wf[k8][nt], of[k8][0], acc[nt][0]);
                acc[nt][1] = mfma32(wf[k8][nt], of[k8][1], acc[nt][1]);
            }
    }
    f32x4* R = (f32x4*)lds;
#pragma unroll
    for (int nt = 0; nt < 4; ++nt) { R[(w * 8 + nt * 2) * 64 + lane] = acc[nt][0]; R[(w * 8 + nt * 2 + 1) * 64 + lane] = acc[nt][1]; }
    __syncthreads();
#pragma unroll
    for (int i = 0; i < 2; ++i) {
        const int tile = w * 2 + i, nt = tile >> 1, mt = tile & 1;
        const f32x4 v = (R[tile * 64 + lane] + R[(8 + tile) * 64 + lane]) + (R[(16 + tile) * 64 + lane] + R[(24 + tile) * 64 + lane]);
        const float v0 = v[0], v1 = v[1], v2 = v[2], v3 = v[3];
        v2u pk;
        pk.x = (unsigned)f2bf(v0) | ((unsigned)f2bf(v1) << 16);
        pk.y = (unsigned)f2bf(v2) | ((unsigned)f2bf(v3) << 16);
        *(v2u*)(a.out + (size_t)(s * SEG + mt * 16 + c16) * (2 * DM) + ns * 64 + nt * 16 + 4 * g) = pk;
    }
    __syncthreads();
}

__device__ __forceinline__ void oproj_fine_task(const Args& a, int s, int n16, unsigned char* lds, gu32* tmo) {
    const int tid = opaque_tid(), w = tid >> 6, lane = tid & 63, c16 = lane & 15, g = lane >> 4;
    const u16* O = (const u16*)(a.ws + O_OFF) + (size_t)(s * SEG + c16) * HD + w * 1024 + 8 * g;
    const u16* Wt = (const u16*)(a.ws + WOT_OFF) + (size_t)(n16 * 16 + c16) * HD + w * 1024 + 8 * g;
    wait_flags(flagp(a.ws, F_TR), 740u, tid == 0, tmo);
    bf16x8 wf[32];
#pragma unroll
    for (int ks = 0; ks < 32; ++ks) wf[ks] = *(const bf16x8*)(Wt + ks * 32);
    wait_flags(flagp(a.ws, F_ATT + s), 8u, tid == 0, tmo);
    f32x4 acc[2] = {(f32x4){0.f, 0.f, 0.f, 0.f}, (f32x4){0.f, 0.f, 0.f, 0.f}};
#pragma unroll
    for (int kb = 0; kb < 2; ++kb) {
        bf16x8 of[16][2];
#pragma unroll
        for (int k = 0; k < 16; ++k) { of[k][0] = *(const bf16x8*)(O + (kb * 16 + k) * 32); of[k][1] = *(const bf16x8*)(O + (size_t)16 * HD + (kb * 16 + k) * 32); }
        __builtin_amdgcn_sched_barrier(0);
#pragma unroll
        for (int k = 0; k < 16; ++k) { acc[0] = mfma32(wf[kb * 16 + k], of[k][0], acc[0]); acc[1] = mfma32(wf[kb * 16 + k], of[k][1], acc[1]); }
    }
    f32x4* R = (f32x4*)lds;
    R[(w * 2) * 64 + lane] = acc[0]; R[(w * 2 + 1) * 64 + lane] = acc[1];
    __syncthreads();
    if (w < 2) {
        const f32x4 v = (R[w * 64 + lane] + R[(2 + w) * 64 + lane]) + (R[(4 + w) * 64 + lane] + R[(6 + w) * 64 + lane]);
        const float v0 = v[0], v1 = v[1], v2 = v[2], v3 = v[3];
        v2u pk;
        pk.x = (unsigned)f2bf(v0) | ((unsigned)f2bf(v1) << 16);
        pk.y = (unsigned)f2bf(v2) | ((unsigned)f2bf(v3) << 16);
        *(v2u*)(a.out + (size_t)(s * SEG + w * 16 + c16) * (2 * DM) + n16 * 16 + 4 * g) = pk;
    }
    __syncthreads();
}

__device__ __forceinline__ void bfinal_task(const Args& a, int rb32, unsigned char* lds) {
    const int tid = opaque_tid(), w = tid >> 6, lane = tid & 63, c16 = lane & 15, g = lane >> 4;
    const u16* Bq = (const u16*)(a.ws + BQKV_OFF);
    constexpr int OST = 1296;
    const float scale = 0.22360679774997896f;
    for (int it = 0; it < 4; ++it) {
        const int idx = it * 256 + tid, h = idx & 31, row = idx >> 5, pos = rb32 * 32 + row;
        const u16* qp = Bq + (size_t)pos * BD3 + h * BDH;
        float q[BDH];
#pragma unroll
        for (int d = 0; d < BDH; d += 2) { const unsigned u = *(const unsigned*)(qp + d); q[d] = bf2f((u16)u); q[d + 1] = bf2f((u16)(u >> 16)); }
        float lg[8]; float mx = -INFINITY;
#pragma unroll
        for (int wi = 0; wi < 8; ++wi) {
            const int kp = pos - 2 * wi;
            float v = -INFINITY;
            if (kp >= 0) {
                const u16* kpn = Bq + (size_t)kp * BD3 + DM + h * BDH;
                float d0 = 0.f;
#pragma unroll
                for (int d = 0; d < BDH; d += 2) { const unsigned u = *(const unsigned*)(kpn + d); d0 += q[d] * bf2f((u16)u); d0 += q[d + 1] * bf2f((u16)(u >> 16)); }
                v = rbf(rbf(d0) * scale);
            }
            lg[wi] = v; mx = fmaxf(mx, v);
        }
        float sum = 0.f;
#pragma unroll
        for (int wi = 0; wi < 8; ++wi) { lg[wi] = __expf(lg[wi] - mx); sum += lg[wi]; }
        const float inv = 1.0f / sum;
        float o[BDH];
#pragma unroll
        for (int d = 0; d < BDH; ++d) o[d] = 0.f;
#pragma unroll
        for (int wi = 0; wi < 8; ++wi) {
            const int kp = pos - 2 * wi;
            if (kp >= 0) {
                const float p = rbf(lg[wi] * inv);
                const u16* vpn = Bq + (size_t)kp * BD3 + 2 * DM + h * BDH;
#pragma unroll
                for (int d = 0; d < BDH; d += 2) { const unsigned u = *(const unsigned*)(vpn + d); o[d] += p * bf2f((u16)u); o[d + 1] += p * bf2f((u16)(u >> 16)); }
            }
        }
#pragma unroll
        for (int d = 0; d < BDH; d += 2) *(unsigned*)(lds + row * OST + (h * BDH + d) * 2) = (unsigned)f2bf(o[d]) | ((unsigned)f2bf(o[d + 1]) << 16);
    }
    __syncthreads();
    const u16* Wt = (const u16*)(a.ws + BWOT_OFF) + (size_t)(w * 160 + c16) * DM + 8 * g;
#pragma unroll 1
    for (int nh = 0; nh < 2; ++nh) {
        f32x4 acc[5][2];
#pragma unroll
        for (int nt = 0; nt < 5; ++nt) { acc[nt][0] = (f32x4){0.f, 0.f, 0.f, 0.f}; acc[nt][1] = (f32x4){0.f, 0.f, 0.f, 0.f}; }
#pragma unroll 4
        for (int ks = 0; ks < 20; ++ks) {
            const bf16x8 o0 = *(const bf16x8*)(lds + c16 * OST + ks * 64 + g * 16), o1 = *(const bf16x8*)(lds + (16 + c16) * OST + ks * 64 + g * 16);
#pragma unroll
            for (int nt = 0; nt < 5; ++nt) {
                const bf16x8 wf = *(const bf16x8*)(Wt + (size_t)(nh * 5 + nt) * 16 * DM + ks * 32);
                acc[nt][0] = mfma32(wf, o0, acc[nt][0]);
                acc[nt][1] = mfma32(wf, o1, acc[nt][1]);
            }
        }
#pragma unroll
        for (int nt = 0; nt < 5; ++nt)
#pragma unroll
            for (int mt = 0; mt < 2; ++mt) {
                const float v0 = acc[nt][mt][0], v1 = acc[nt][mt][1], v2 = acc[nt][mt][2], v3 = acc[nt][mt][3];
                v2u pk;
                pk.x = (unsigned)f2bf(v0) | ((unsigned)f2bf(v1) << 16);
                pk.y = (unsigned)f2bf(v2) | ((unsigned)f2bf(v3) << 16);
                *(v2u*)(a.out + (size_t)(rb32 * 32 + mt * 16 + c16) * (2 * DM) + DM + w * 160 + (nh * 5 + nt) * 16 + 4 * g) = pk;
            }
    }
    __syncthreads();
}

constexpr int NWORK = 192;
constexpr int T_KV = 0, T_TR = T_KV + 1024, T_Q = T_TR + 740, T_BQ = T_Q + 512, T_BF = T_BQ + 240, T_SEG = T_BF + 64, T_LAST = T_SEG + 60 * 18, T_END = T_LAST + 4 * 48;

__device__ __forceinline__ void worker_role(const Args& a, unsigned char* lds) {
    const int wid = blockIdx.x - 64, tid = threadIdx.x, w = tid >> 6;
    gu32* tmo = (gu32*)(a.ws + TMO_OFF);
    u16* Q = (u16*)(a.ws + Q_OFF); u16* K = (u16*)(a.ws + K_OFF); u16* Vt = (u16*)(a.ws + VT_OFF); u16* Bq = (u16*)(a.ws + BQKV_OFF);
    for (int T = wid; T < T_END; T += NWORK) {
        if (T < T_TR) {
            const int rb = T >> 6, r = T & 63, hc = r & 31;
            if (r < 32) gemm_tile<0>(a.Ax, DM, a.Wk, HD, K, HD, DM, rb * 128, hc * 128, lds);
            else gemm_tile<1>(a.Ax, DM, a.Wv, HD, Vt, HD, DM, rb * 128, hc * 128, lds);
            signal_flag(flagp(a.ws, F_KV + rb * 32 + hc));
        } else if (T < T_Q) {
            const int t = T - T_TR;
            if (t < 640) transpose_tile(a.Wo, DM, (u16*)(a.ws + WOT_OFF), HD, (t / 10) * 64, (t % 10) * 64, lds, a.ws);
            else transpose_tile(a.Bwo, DM, (u16*)(a.ws + BWOT_OFF), DM, ((t - 640) / 10) * 64, ((t - 640) % 10) * 64, lds, a.ws);
            signal_flag(flagp(a.ws, F_TR));
        } else if (T < T_BQ) {
            const int t = T - T_Q, rb = t >> 5, hc = t & 31;
            gemm_tile<0>(a.Ax, DM, a.Wq, HD, Q, HD, DM, rb * 128, hc * 128, lds);
            signal_flag(flagp(a.ws, F_Q + rb * 32 + hc));
        } else if (T < T_BF) {
            const int t = T - T_BQ, rb = t / 15, nt = t - rb * 15;
            gemm_tile<0>(a.Bx, DM, a.Bw, BD3, Bq, BD3, DM, rb * 128, nt * 128, lds);
            signal_flag(flagp(a.ws, F_BQ + rb));
        } else if (T < T_SEG) {
            const int rb32 = T - T_BF, rb = rb32 >> 2;
            const int fi = tid == 0 ? F_BQ + rb : (tid == 1 ? F_BQ + (rb > 0 ? rb - 1 : 0) : F_TR);
            wait_flags(flagp(a.ws, fi), tid < 2 ? 15u : 740u, tid < 3, tmo);
            bfinal_task(a, rb32, lds);
        } else {
            const int t = T - T_SEG, s = T < T_LAST ? t / 18 : 60 + (T - T_LAST) / 48, r = T < T_LAST ? t - s * 18 : (T - T_LAST) % 48, rb = s >> 2;
            if (T >= T_LAST && r >= 8) {
                oproj_fine_task(a, s, r - 8, lds, tmo);
            } else if (r < 8) {
                const int hh = r * 4 + (tid & 3), kind = (tid >> 2) & 3;
                const int fi = kind == 0 ? F_KV + rb * 32 + hh : (kind == 1 ? F_Q + rb * 32 + hh : F_KM + s * 32 + hh);
                wait_flags(flagp(a.ws, fi), kind == 1 ? 1u : 2u, tid < 12, tmo);
                attn_x(a, s, r * 4 + w);
                signal_flag(flagp(a.ws, F_ATT + s));
            } else {
                wait_flags(flagp(a.ws, tid == 0 ? F_ATT + s : F_TR), tid == 0 ? 8u : 740u, tid < 2, tmo);
                oproj_task(a, s, r - 8, lds);
            }
        }
    }
}

__global__ __launch_bounds__(256, 1) void fused_kernel(Args a) {
    extern __shared__ __attribute__((aligned(16))) unsigned char lds[];
    if (blockIdx.x < 64) chain_role(a, lds);
    else worker_role(a, lds);
}

extern "C" void kernel_launch(void* const* d_in, const int* in_sizes, int n_in,
                              void* d_out, int out_size, void* d_ws, size_t ws_size,
                              hipStream_t stream) {
    const u16* Ax = (const u16*)d_in[0];
    const u16* mem0 = (const u16*)d_in[1];
    const u16* Wq = (const u16*)d_in[2];
    const u16* Wk = (const u16*)d_in[3];
    const u16* Wv = (const u16*)d_in[4];
    const u16* Wo = (const u16*)d_in[5];
    const u16* Bx = (const u16*)d_in[7];
    const u16* Bw = (const u16*)d_in[8];
    const u16* Bwo = (const u16*)d_in[9];
    unsigned char* ws = (unsigned char*)d_ws;
    u16* out = (u16*)d_out;

    (void)hipMemsetAsync(ws, 0, XCHG_BYTES, stream);
    Args a{Ax, mem0, Wq, Wk, Wv, Wo, Bx, Bw, Bwo, out, ws};
    (void)hipFuncSetAttribute((const void*)fused_kernel, hipFuncAttributeMaxDynamicSharedMemorySize, CHAIN_LDS);
    fused_kernel<<<256, 256, CHAIN_LDS, stream>>>(a);
}
```

```cpp
#include <hip/hip_runtime.h>
#include <hip/hip_bf16.h>
#include <stdint.h>

#define GAS __attribute__((address_space(1)))
#define LAS __attribute__((address_space(3)))
typedef unsigned short u16;
typedef short bf16x8 __attribute__((ext_vector_type(8)));
typedef short bf16x4 __attribute__((ext_vector_type(4)));
typedef float f32x4 __attribute__((ext_vector_type(4)));
typedef unsigned v4u __attribute__((ext_vector_type(4)));
typedef unsigned v2u __attribute__((ext_vector_type(2)));
typedef GAS unsigned long long gu64;
typedef GAS unsigned gu32;
typedef LAS bf16x4 lds_bf16x4;
#define RLX_AGENT __ATOMIC_RELAXED, __HIP_MEMORY_SCOPE_AGENT

constexpr int SEQ = 2048, DM = 640, NH = 32, DH = 128, MEMN = 8, SEG = 32, NSEG = 64, HD = 4096;
constexpr int BD3 = 1920, BDH = 20;

constexpr size_t XA_OFF = 0;
constexpr size_t XA_BYTES = (size_t)2 * 64 * 512 * 8;
constexpr size_t XB_OFF = XA_OFF + XA_BYTES;
constexpr size_t XB_BYTES = (size_t)2 * 64 * 2560 * 8;
constexpr size_t XC_OFF = XB_OFF + XB_BYTES;
constexpr int XC_REP = 1;
constexpr size_t XC_BYTES = (size_t)2 * XC_REP * 2560 * 8;
constexpr size_t TMO_OFF = XC_OFF + XC_BYTES;
constexpr size_t FL_OFF = TMO_OFF + 256;
constexpr int F_KV = 0, F_Q = F_KV + 512, F_KM = F_Q + 512, F_ATT = F_KM + 2048, F_BQ = F_ATT + 64, F_TR = F_BQ + 16, F_END = F_TR + 1;
constexpr size_t XCHG_BYTES = (FL_OFF + (size_t)F_END * 64 + 255) / 256 * 256;
constexpr size_t Q_OFF = (XCHG_BYTES + 4095) / 4096 * 4096;
constexpr size_t K_OFF = Q_OFF + (size_t)SEQ * HD * 2;
constexpr size_t VT_OFF = K_OFF + (size_t)SEQ * HD * 2;
constexpr size_t KM_OFF = VT_OFF + (size_t)SEQ * HD * 2;
constexpr size_t VM_OFF = KM_OFF + (size_t)NSEG * MEMN * HD * 2;
constexpr size_t O_OFF = VM_OFF + (size_t)NSEG * MEMN * HD * 2;
constexpr size_t BQKV_OFF = O_OFF + (size_t)SEQ * HD * 2;
constexpr size_t WOT_OFF = BQKV_OFF + (size_t)SEQ * BD3 * 2;
constexpr size_t BWOT_OFF = WOT_OFF + (size_t)DM * HD * 2;
constexpr size_t WS_END = BWOT_OFF + (size_t)DM * DM * 2;

__device__ __forceinline__ int opaque_tid() { int t = threadIdx.x; asm volatile("" : "+v"(t)); return t; }
__device__ __forceinline__ u16 f2bf(float f) { return __builtin_bit_cast(u16, (__bf16)f); }
__device__ __forceinline__ float bf2f(u16 b) { return __builtin_bit_cast(float, (unsigned)b << 16); }
__device__ __forceinline__ float rbf(float f) { return bf2f(f2bf(f)); }
__device__ __forceinline__ f32x4 mfma32(bf16x8 a, bf16x8 b, f32x4 c) { return __builtin_amdgcn_mfma_f32_16x16x32_bf16(a, b, c, 0, 0, 0); }

__device__ __forceinline__ void st8(u16* p, v2u v) { __hip_atomic_store((gu64*)p, (unsigned long long)v.x | ((unsigned long long)v.y << 32), RLX_AGENT); }
__device__ __forceinline__ unsigned off_b(unsigned row, unsigned ch) { return 256u * row + 16u * (ch ^ (((row & 3) << 2) | ((row >> 2) & 3))); }

template <int MODE>
__device__ __forceinline__ void gemm_tile(const u16* __restrict__ A, int lda, const u16* __restrict__ W, int ldw,
                                          u16* __restrict__ C, int ldc, int K, int m0, int n0, unsigned char* lds) {
    const int tid = opaque_tid(), w = tid >> 6, lane = tid & 63, c16 = lane & 15, g = lane >> 4;
    const int wm = w >> 1, wn = w & 1;
    unsigned char* LA = lds;
    unsigned char* LW = lds + 32768;
    f32x4 acc[4][4];
#pragma unroll
    for (int i = 0; i < 4; ++i)
#pragma unroll
        for (int jn = 0; jn < 4; ++jn) acc[i][jn] = (f32x4){0.f, 0.f, 0.f, 0.f};
    constexpr int NK = 10, RD = 4;
    v4u ra[RD][4], rw[RD][4];
    const u16* Ap = A + (size_t)(m0 + (tid >> 3)) * lda + (tid & 7) * 8;
    const u16* Wp = W + (size_t)(tid >> 4) * ldw + n0 + (tid & 15) * 8;
    const unsigned q4 = (lane & 15) >> 2, p4 = lane & 3;
#pragma unroll
    for (int kt = 0; kt < RD; ++kt)
#pragma unroll
        for (int i = 0; i < 4; ++i) {
            ra[kt][i] = *(const v4u*)(Ap + (size_t)i * 32 * lda + kt * 64);
            rw[kt][i] = *(const v4u*)(Wp + (size_t)(kt * 64 + i * 16) * ldw);
        }
#pragma unroll
    for (int kt = 0; kt < NK; ++kt) {
        const int buf = kt & 1, sl = kt % RD;
#pragma unroll
        for (int i = 0; i < 4; ++i) {
            const int c = tid + 256 * i;
            const int row = c >> 3, ch = c & 7;
            *(v4u*)(LA + buf * 16384 + row * 128 + 16 * (ch ^ (row & 7))) = ra[sl][i];
            const int r2 = c >> 4, ch2 = c & 15;
            *(v4u*)(LW + buf * 16384 + (r2 >> 5) * 8192 + off_b(r2 & 31, ch2)) = rw[sl][i];
        }
        __syncthreads();
        if (kt + RD < NK) {
#pragma unroll
            for (int i = 0; i < 4; ++i) {
                ra[sl][i] = *(const v4u*)(Ap + (size_t)i * 32 * lda + (kt + RD) * 64);
                rw[sl][i] = *(const v4u*)(Wp + (size_t)((kt + RD) * 64 + i * 16) * ldw);
            }
        }
#pragma unroll
        for (int ks = 0; ks < 2; ++ks) {
            bf16x8 af[4], wf[4];
#pragma unroll
            for (int mt = 0; mt < 4; ++mt) {
                const int row = wm * 64 + mt * 16 + c16, ch = ks * 4 + g;
                af[mt] = *(const bf16x8*)(LA + buf * 16384 + row * 128 + 16 * (ch ^ (row & 7)));
            }
#pragma unroll
            for (int nt = 0; nt < 4; ++nt) {
                const unsigned cb = wn * 4 + nt;
                unsigned char* base = LW + buf * 16384 + ks * 8192;
                const bf16x4 lo = __builtin_amdgcn_ds_read_tr16_b64_v4i16((lds_bf16x4*)(base + off_b(8 * g + q4, 2 * cb + (p4 >> 1)) + 8 * (p4 & 1)));
                const bf16x4 hi = __builtin_amdgcn_ds_read_tr16_b64_v4i16((lds_bf16x4*)(base + off_b(8 * g + 4 + q4, 2 * cb + (p4 >> 1)) + 8 * (p4 & 1)));
                wf[nt] = __builtin_shufflevector(lo, hi, 0, 1, 2, 3, 4, 5, 6, 7);
            }
#pragma unroll
            for (int mt = 0; mt < 4; ++mt)
#pragma unroll
                for (int nt = 0; nt < 4; ++nt)
                    acc[mt][nt] = (MODE == 0) ? mfma32(wf[nt], af[mt], acc[mt][nt]) : mfma32(af[mt], wf[nt], acc[mt][nt]);
        }
    }
    __syncthreads();
#pragma unroll
    for (int mt = 0; mt < 4; ++mt)
#pragma unroll
        for (int nt = 0; nt < 4; ++nt) {
            v2u pk;
            pk.x = (unsigned)f2bf(acc[mt][nt][0]) | ((unsigned)f2bf(acc[mt][nt][1]) << 16);
            pk.y = (unsigned)f2bf(acc[mt][nt][2]) | ((unsigned)f2bf(acc[mt][nt][3]) << 16);
            if (MODE == 0) {
                const int m = m0 + wm * 64 + mt * 16 + c16, n = n0 + wn * 64 + nt * 16 + 4 * g;
                st8(C + (size_t)m * ldc + n, pk);
            } else {
                const int m = m0 + wm * 64 + mt * 16 + 4 * g, n = n0 + wn * 64 + nt * 16 + c16;
                st8(C + ((size_t)(m >> 5) * HD + n) * 32 + 8 * g + 4 * ((m >> 4) & 1), pk);
            }
        }
}

struct Args { const u16* Ax; const u16* mem0; const u16* Wq; const u16* Wk; const u16* Wv; const u16* Wo;
              const u16* Bx; const u16* Bw; const u16* Bwo; u16* out; unsigned char* ws; };

constexpr unsigned kSpinCap = 1u << 17;
#define SLEEP_A 0
#define SLEEP_B 16
#define SLEEP_C 16

typedef __amdgpu_buffer_rsrc_t rsrc_t;
__device__ __forceinline__ void st16(rsrc_t rs, int off, v4u v) { __builtin_amdgcn_raw_buffer_store_b128(v, rs, off, 0, 16); }
template <int NPT, bool TAG16>
__device__ __forceinline__ void gather(rsrc_t rs, const int (&off)[NPT], unsigned tag, v4u (&v)[NPT], gu32* tmo) {
    for (unsigned spins = 0;; ++spins) {
        asm volatile("" ::: "memory");
#pragma unroll
        for (int k = 0; k < NPT; ++k) v[k] = __builtin_amdgcn_raw_buffer_load_b128(rs, off[k], 0, 16);
        bool ok = true;
#pragma unroll
        for (int k = 0; k < NPT; ++k) {
            if (TAG16) ok &= ((v[k].y >> 16) == tag) & ((v[k].w >> 16) == tag);
            else ok &= (v[k].y == tag) & (v[k].w == tag);
        }
        if (__all(ok)) break;
        if ((spins & 63u) == 63u) {
            if (spins > kSpinCap) { __hip_atomic_store(tmo, 1u, RLX_AGENT); break; }
            if (__hip_atomic_load(tmo, RLX_AGENT) != 0u) break;
        }
    }
}
__device__ __forceinline__ float xmax16_32(float v) {
    auto r = __builtin_amdgcn_permlane16_swap(__float_as_uint(v), __float_as_uint(v), false, false);
    v = fmaxf(__uint_as_float(r[0]), __uint_as_float(r[1]));
    auto r2 = __builtin_amdgcn_permlane32_swap(__float_as_uint(v), __float_as_uint(v), false, false);
    return fmaxf(__uint_as_float(r2[0]), __uint_as_float(r2[1]));
}
__device__ __forceinline__ float xsum16_32(float v) {
    auto r = __builtin_amdgcn_permlane16_swap(__float_as_uint(v), __float_as_uint(v), false, false);
    v = __uint_as_float(r[0]) + __uint_as_float(r[1]);
    auto r2 = __builtin_amdgcn_permlane32_swap(__float_as_uint(v), __float_as_uint(v), false, false);
    return __uint_as_float(r2[0]) + __uint_as_float(r2[1]);
}

constexpr int MEM_STRIDE = 1296, QK_STRIDE = 272;
constexpr int L_WO = 0;
constexpr int L_MEM = L_WO + 81920, L_Q = L_MEM + 8 * MEM_STRIDE, L_K = L_Q + 8 * QK_STRIDE, L_VT = L_K + 8 * QK_STRIDE,
              L_RED = L_VT + 128 * 32, L_KV = L_RED + 6 * 80 * 4, L_END = L_KV + 16 * 1024;
constexpr int L_STAGE = L_MEM;
constexpr int CHAIN_LDS = (L_STAGE + 51200 > L_END) ? (L_STAGE + 51200) : L_END;

__device__ __forceinline__ gu32* flagp(unsigned char* ws, int idx) { return (gu32*)(ws + FL_OFF + (size_t)idx * 64); }

__device__ __forceinline__ void chain_role(const Args& a, unsigned char* lds) {
    const int cu = blockIdx.x, h = cu >> 1, j = cu & 1;
    const int tid = threadIdx.x, w = tid >> 6, lane = tid & 63, c16 = lane & 15, g = lane >> 4;
    gu32* tmo = (gu32*)(a.ws + TMO_OFF);
    const rsrc_t xr = __builtin_amdgcn_make_buffer_rsrc((void*)a.ws, 0, (int)TMO_OFF, 0x00020000);
    const rsrc_t kr_ = __builtin_amdgcn_make_buffer_rsrc((void*)(a.ws + K_OFF), 0, SEQ * HD * 2, 0x00020000);
    const rsrc_t vr_ = __builtin_amdgcn_make_buffer_rsrc((void*)(a.ws + VT_OFF), 0, SEQ * HD * 2, 0x00020000);
    u16* Km = (u16*)(a.ws + KM_OFF);
    u16* Vm = (u16*)(a.ws + VM_OFF);

    bf16x8 wqkv[3][20];
    {
        unsigned char* stg = lds + L_STAGE;
        const u16* Wqk = j ? a.Wk : a.Wq;
        const unsigned q4 = (lane & 15) >> 2, p4 = lane & 3;
#pragma unroll
        for (int rd = 0; rd < 5; ++rd) {
            v4u tmp[12];
#pragma unroll
            for (int i = 0; i < 12; ++i) {
                const int c = tid + 256 * i;
                const int row = c / 24, ch = c - row * 24;
                const size_t kr = (size_t)(rd * 128 + row) * HD + h * DH;
                tmp[i] = (ch < 16) ? *(const v4u*)(Wqk + kr + ch * 8) : *(const v4u*)(a.Wv + kr + j * 64 + (ch - 16) * 8);
            }
#pragma unroll
            for (int i = 0; i < 12; ++i) {
                const int c = tid + 256 * i;
                const int row = c / 24, ch = c - row * 24;
                *(v4u*)(stg + row * 400 + ch * 16) = tmp[i];
            }
            __syncthreads();
#pragma unroll
            for (int k4 = 0; k4 < 4; ++k4)
#pragma unroll
                for (int t = 0; t < 3; ++t) {
                    const int col = (t < 2) ? ((2 * w + t) * 16) : (128 + w * 16);
                    unsigned char* p0 = stg + (k4 * 32 + 8 * g + q4) * 400 + (col + 4 * p4) * 2;
                    const bf16x4 lo = __builtin_amdgcn_ds_read_tr16_b64_v4i16((lds_bf16x4*)p0);
                    const bf16x4 hi = __builtin_amdgcn_ds_read_tr16_b64_v4i16((lds_bf16x4*)(p0 + 4 * 400));
                    wqkv[t][rd * 4 + k4] = __builtin_shufflevector(lo, hi, 0, 1, 2, 3, 4, 5, 6, 7);
                }
            __syncthreads();
        }
#pragma unroll
        for (int rd = 0; rd < 2; ++rd) {
            v4u tmp[10];
#pragma unroll
            for (int i = 0; i < 10; ++i) {
                const int c = tid + 256 * i;
                const int row = c / 40, ch = c - row * 40;
                tmp[i] = *(const v4u*)(a.Wo + (size_t)(h * DH + rd * 64 + row) * DM + j * 320 + ch * 8);
            }
#pragma unroll
            for (int i = 0; i < 10; ++i) {
                const int c = tid + 256 * i;
                const int row = c / 40, ch = c - row * 40;
                *(v4u*)(stg + row * 656 + ch * 16) = tmp[i];
            }
            __syncthreads();
#pragma unroll
            for (int k2 = 0; k2 < 2; ++k2)
#pragma unroll
                for (int nt = 0; nt < 5; ++nt) {
                    unsigned char* p0 = stg + (k2 * 32 + 4 * g + q4) * 656 + (w * 80 + nt * 16 + 4 * p4) * 2;
                    const bf16x4 lo = __builtin_amdgcn_ds_read_tr16_b64_v4i16((lds_bf16x4*)p0);
                    const bf16x4 hi = __builtin_amdgcn_ds_read_tr16_b64_v4i16((lds_bf16x4*)(p0 + 16 * 656));
                    *(bf16x8*)(lds + L_WO + ((w * 5 + nt) * 4 + rd * 2 + k2) * 1024 + lane * 16) = __builtin_shufflevector(lo, hi, 0, 1, 2, 3, 4, 5, 6, 7);
                }
            __syncthreads();
        }
        if (tid < 128) *(v4u*)(lds + L_VT + tid * 32 + 16) = (v4u){0u, 0u, 0u, 0u};
    }
    const float scale = 0.08838834764831845f;
    const int tt = tid < 240 ? tid : 239, hg = tt / 40, pr = tt - hg * 40;

    for (int s = 0; s < NSEG; ++s) {
        const unsigned tag = (unsigned)(s + 1);
        const int par = s & 1;
        if ((s & 3) == 0) {
            gu32* fl = flagp(a.ws, F_KV + (s >> 2) * 32 + h);
            for (unsigned spins = 0;; ++spins) {
                if (__builtin_amdgcn_readfirstlane(__hip_atomic_load(fl, RLX_AGENT)) >= 2u) break;
                __builtin_amdgcn_s_sleep(4);
                if ((spins & 63u) == 63u) {
                    if (spins > kSpinCap) { __hip_atomic_store(tmo, 1u, RLX_AGENT); break; }
                    if (__hip_atomic_load(tmo, RLX_AGENT) != 0u) break;
                }
            }
            __builtin_amdgcn_fence(__ATOMIC_ACQUIRE, "wavefront");
        }
        v4u kvq[4];
#pragma unroll
        for (int i = 0; i < 4; ++i) {
            if (w < 2) kvq[i] = __builtin_amdgcn_raw_buffer_load_b128(kr_, ((s * SEG + w * 16 + c16) * HD + h * DH + 32 * i + 8 * g) * 2, 0, 16);
            else kvq[i] = __builtin_amdgcn_raw_buffer_load_b128(vr_, ((s * HD + h * DH + ((w - 2) * 4 + i) * 16 + c16) * 32 + 8 * g) * 2, 0, 16);
        }
        if (s == 0) {
            for (int i = tid; i < 8 * 80; i += 256) {
                const int tk = i / 80, ch = i % 80;
                *(v4u*)(lds + L_MEM + tk * MEM_STRIDE + ch * 16) = *(const v4u*)(a.mem0 + tk * DM + ch * 8);
            }
        } else {
            v4u v[5];
            __builtin_amdgcn_s_sleep(SLEEP_C);
            const int o0 = (int)XC_OFF + (((s - 1) & 1) * XC_REP + (cu & (XC_REP - 1))) * 20480 + tid * 16;
            const int offs[5] = {o0, o0 + 4096, o0 + 8192, o0 + 12288, o0 + 16384};
            gather<5, false>(xr, offs, (unsigned)s, v, tmo);
            {
                const int l2 = (2 * tid) & 63;
                const int tok = l2 & 7, col = ((tid >> 5) * 16 + (l2 >> 4) * 4 + ((l2 >> 3) & 1) * 2) * 2;
#pragma unroll
                for (int k = 0; k < 5; ++k) {
                    *(unsigned*)(lds + L_MEM + tok * MEM_STRIDE + col + k * 256) = v[k].x;
                    *(unsigned*)(lds + L_MEM + (tok + 1) * MEM_STRIDE + col + k * 256) = v[k].z;
                }
            }
        }
#pragma unroll
        for (int i = 0; i < 4; ++i) *(v4u*)(lds + L_KV + (w * 4 + i) * 1024 + lane * 16) = kvq[i];
        __syncthreads();
        f32x4 acc[3];
        {
            bf16x8 mb[20];
#pragma unroll
            for (int ks = 0; ks < 20; ++ks) mb[ks] = *(const bf16x8*)(lds + L_MEM + (c16 & 7) * MEM_STRIDE + ks * 64 + g * 16);
            __builtin_amdgcn_sched_barrier(0);
#pragma unroll
            for (int t = 0; t < 3; ++t) acc[t] = (f32x4){0.f, 0.f, 0.f, 0.f};
#pragma unroll
            for (int ks = 0; ks < 20; ++ks)
#pragma unroll
                for (int t = 0; t < 3; ++t) acc[t] = mfma32(wqkv[t][ks], mb[ks], acc[t]);
        }
        u16 qv[3][4];
#pragma unroll
        for (int t = 0; t < 3; ++t)
#pragma unroll
            for (int r = 0; r < 4; ++r) { const float f = acc[t][r]; qv[t][r] = f2bf(f); }
        if (s + 1 < NSEG) {
            const bool hi = c16 >= 8;
            v4u pk;
            pk.x = (unsigned)(hi ? qv[0][2] : qv[0][0]) | ((unsigned)(hi ? qv[1][2] : qv[1][0]) << 16);
            pk.y = (unsigned)(hi ? qv[2][2] : qv[2][0]) | (tag << 16);
            pk.z = (unsigned)(hi ? qv[0][3] : qv[0][1]) | ((unsigned)(hi ? qv[1][3] : qv[1][1]) << 16);
            pk.w = (unsigned)(hi ? qv[2][3] : qv[2][1]) | (tag << 16);
            st16(xr, (int)XA_OFF + (par * 64 + cu) * 4096 + tid * 16, pk);
        }
        if (c16 < 8) {
            const int token = c16;
            unsigned char* qk = lds + (j ? L_K : L_Q) + token * QK_STRIDE;
#pragma unroll
            for (int t = 0; t < 2; ++t) {
                v2u pk; pk.x = (unsigned)qv[t][0] | ((unsigned)qv[t][1] << 16); pk.y = (unsigned)qv[t][2] | ((unsigned)qv[t][3] << 16);
                *(v2u*)(qk + ((2 * w + t) * 16 + 4 * g) * 2) = pk;
            }
#pragma unroll
            for (int r = 0; r < 4; ++r) {
                const int e = j * 64 + w * 16 + 4 * g + r;
                *(u16*)(lds + L_VT + e * 32 + token * 2) = qv[2][r];
            }
        }
        auto store_kv = [&]() {
            if (c16 < 8) {
                if (j) {
#pragma unroll
                    for (int t = 0; t < 2; ++t) {
                        v2u pk; pk.x = (unsigned)qv[t][0] | ((unsigned)qv[t][1] << 16); pk.y = (unsigned)qv[t][2] | ((unsigned)qv[t][3] << 16);
                        st8(Km + (size_t)(s * MEMN + c16) * HD + h * DH + (2 * w + t) * 16 + 4 * g, pk);
                    }
                }
                v2u pk; pk.x = (unsigned)qv[2][0] | ((unsigned)qv[2][1] << 16); pk.y = (unsigned)qv[2][2] | ((unsigned)qv[2][3] << 16);
                st8(Vm + (size_t)(s * MEMN + c16) * HD + h * DH + j * 64 + w * 16 + 4 * g, pk);
            }
        };
        if (s + 1 == NSEG) {
            store_kv();
            asm volatile("s_waitcnt vmcnt(0)" ::: "memory");
            __syncthreads();
            if (tid == 0) __hip_atomic_fetch_add(flagp(a.ws, F_KM + s * 32 + h), 1u, RLX_AGENT);
        }
        if (s + 1 == NSEG) break;
        bf16x8 kx[2][4], vx[8];
#pragma unroll
        for (int f8 = 0; f8 < 8; ++f8) kx[f8 >> 2][f8 & 3] = *(const bf16x8*)(lds + L_KV + f8 * 1024 + lane * 16);
#pragma unroll
        for (int f8 = 0; f8 < 8; ++f8) vx[f8] = *(const bf16x8*)(lds + L_KV + (8 + f8) * 1024 + lane * 16);
        {
            v4u v[1];
            __builtin_amdgcn_s_sleep(SLEEP_A);
            const int offs[1] = {(int)XA_OFF + (par * 64 + (cu ^ 1)) * 4096 + tid * 16};
            gather<1, true>(xr, offs, tag, v, tmo);
            const int pj = j ^ 1;
            const int pw = tid >> 6, pg = (tid >> 4) & 3, token = tid & 7, r = ((tid >> 3) & 1) * 2;
            unsigned char* qk = lds + (pj ? L_K : L_Q) + token * QK_STRIDE;
            *(unsigned*)(qk + ((2 * pw) * 16 + 4 * pg + r) * 2) = (v[0].x & 0xffffu) | (v[0].z << 16);
            *(unsigned*)(qk + ((2 * pw + 1) * 16 + 4 * pg + r) * 2) = (v[0].x >> 16) | (v[0].z & 0xffff0000u);
            *(u16*)(lds + L_VT + (pj * 64 + pw * 16 + 4 * pg + r) * 32 + token * 2) = (u16)v[0].y;
            *(u16*)(lds + L_VT + (pj * 64 + pw * 16 + 4 * pg + r + 1) * 32 + token * 2) = (u16)v[0].w;
        }
        __syncthreads();
        store_kv();
        bf16x8 qf[4], kf[4];
        bf16x4 vm4[8];
#pragma unroll
        for (int ks = 0; ks < 4; ++ks) {
            qf[ks] = *(const bf16x8*)(lds + L_Q + (c16 & 7) * QK_STRIDE + ks * 64 + g * 16);
            kf[ks] = *(const bf16x8*)(lds + L_K + (c16 & 7) * QK_STRIDE + ks * 64 + g * 16);
        }
#pragma unroll
        for (int et = 0; et < 8; ++et) vm4[et] = *(const bf16x4*)(lds + L_VT + (et * 16 + c16) * 32 + g * 8);
        __builtin_amdgcn_sched_barrier(0);
        f32x4 st[3];
#pragma unroll
        for (int t = 0; t < 3; ++t) st[t] = (f32x4){0.f, 0.f, 0.f, 0.f};
#pragma unroll
        for (int ks = 0; ks < 4; ++ks) {
            st[1] = mfma32(kx[0][ks], qf[ks], st[1]);
            st[2] = mfma32(kx[1][ks], qf[ks], st[2]);
            st[0] = mfma32(kf[ks], qf[ks], st[0]);
        }
        __builtin_amdgcn_sched_barrier(0);
        bf16x8 wo[5][4];
#pragma unroll
        for (int nt = 0; nt < 5; ++nt)
#pragma unroll
            for (int ks = 0; ks < 4; ++ks) wo[nt][ks] = *(const bf16x8*)(lds + L_WO + ((w * 5 + nt) * 4 + ks) * 1024 + lane * 16);
        __builtin_amdgcn_sched_barrier(0);
        float x[3][4];
        float mx = -INFINITY;
#pragma unroll
        for (int t = 0; t < 3; ++t)
#pragma unroll
            for (int r = 0; r < 4; ++r) {
                const float sv = st[t][r];
                float v = rbf(rbf(sv) * scale);
                if (t == 0 && g >= 2) v = -INFINITY;
                x[t][r] = v; mx = fmaxf(mx, v);
            }
        mx = xmax16_32(mx);
        float sum = 0.f;
#pragma unroll
        for (int t = 0; t < 3; ++t)
#pragma unroll
            for (int r = 0; r < 4; ++r) { x[t][r] = __expf(x[t][r] - mx); sum += x[t][r]; }
        sum = xsum16_32(sum);
        const float inv = __builtin_amdgcn_rcpf(sum);
        bf16x8 px, pm;
#pragma unroll
        for (int r = 0; r < 4; ++r) {
            pm[r] = (short)f2bf(x[0][r] * inv); pm[4 + r] = 0;
            px[r] = (short)f2bf(x[1][r] * inv);
            px[4 + r] = (short)f2bf(x[2][r] * inv);
        }
        f32x4 oa[8];
#pragma unroll
        for (int et = 0; et < 8; ++et) oa[et] = mfma32(vx[et], px, (f32x4){0.f, 0.f, 0.f, 0.f});
#pragma unroll
        for (int et = 0; et < 8; ++et) {
            const bf16x8 vm = __builtin_shufflevector(vm4[et], (bf16x4){0, 0, 0, 0}, 0, 1, 2, 3, 4, 5, 6, 7);
            oa[et] = mfma32(vm, pm, oa[et]);
        }
        bf16x8 of[4];
#pragma unroll
        for (int et = 0; et < 8; ++et)
#pragma unroll
            for (int r = 0; r < 4; ++r) { const float f = oa[et][r]; of[et >> 1][(et & 1) * 4 + r] = (short)f2bf(f); }
        {
            f32x4 p[5];
#pragma unroll
            for (int nt = 0; nt < 5; ++nt) p[nt] = (f32x4){0.f, 0.f, 0.f, 0.f};
#pragma unroll
            for (int ks = 0; ks < 4; ++ks)
#pragma unroll
                for (int nt = 0; nt < 5; ++nt) p[nt] = mfma32(wo[nt][ks], of[ks], p[nt]);
            const int off = (int)XB_OFF + (par * 32 * 2560 + h * 2560 + (j * 4 + w) * 320 + lane) * 16;
            const bool hi = c16 >= 8;
#pragma unroll
            for (int nt = 0; nt < 5; ++nt) {
                const float p0 = p[nt][0], p1 = p[nt][1], p2 = p[nt][2], p3 = p[nt][3];
                v4u pk; pk.x = __float_as_uint(hi ? p2 : p0); pk.y = tag; pk.z = __float_as_uint(hi ? p3 : p1); pk.w = tag;
                st16(xr, off + nt * 1024, pk);
            }
        }
        {
            const int off = (int)XB_OFF + (par * 32 * 2560 + hg * 2560 + cu * 40 + pr) * 16;
            const int off5 = off + (hg < 2 ? 5 : 4) * 6 * 2560 * 16;
            v4u v[6];
            __builtin_amdgcn_s_sleep(SLEEP_B);
            constexpr int HS = 6 * 2560 * 16;
            const int offs[6] = {off, off + HS, off + 2 * HS, off + 3 * HS, off + 4 * HS, off5};
            gather<6, false>(xr, offs, tag, v, tmo);
            float s0 = 0.f, s1 = 0.f;
#pragma unroll
            for (int k = 0; k < 5; ++k) { s0 += __uint_as_float(v[k].x); s1 += __uint_as_float(v[k].z); }
            if (hg < 2) { s0 += __uint_as_float(v[5].x); s1 += __uint_as_float(v[5].z); }
            if (tid < 240) { v2u pk; pk.x = __float_as_uint(s0); pk.y = __float_as_uint(s1); *(v2u*)(lds + L_RED + tid * 8) = pk; }
        }
        asm volatile("s_waitcnt vmcnt(0)" ::: "memory");
        __syncthreads();
        if (tid == 192) __hip_atomic_fetch_add(flagp(a.ws, F_KM + s * 32 + h), 1u, RLX_AGENT);
        if (tid < 128) {
            const int t80 = tid < 80 ? tid : 79;
            const float* rp = (const float*)(lds + L_RED) + t80;
            const float sacc = ((rp[0] + rp[80]) + (rp[160] + rp[240])) + (rp[320] + rp[400]);
            const unsigned mine = f2bf(sacc);
            const unsigned m1 = (unsigned)__builtin_amdgcn_mov_dpp((int)mine, 0x55, 0xf, 0xf, true);
            const unsigned m2 = (unsigned)__builtin_amdgcn_mov_dpp((int)mine, 0xAA, 0xf, 0xf, true);
            const unsigned m3 = (unsigned)__builtin_amdgcn_mov_dpp((int)mine, 0xFF, 0xf, 0xf, true);
            if (tid < 80 && (tid & 3) == 0) {
                v4u pk; pk.x = mine | (m1 << 16); pk.y = tag; pk.z = m2 | (m3 << 16); pk.w = tag;
#pragma unroll
                for (int rp = 0; rp < XC_REP; ++rp) st16(xr, (int)XC_OFF + (par * XC_REP + rp) * 20480 + (cu * 20 + (tid >> 2)) * 16, pk);
            }
        }
    }
}

__device__ __forceinline__ void wait_flags(gu32* myflag, unsigned target, bool active, gu32* tmo) {
    if (threadIdx.x < 64) {
        for (unsigned spins = 0;; ++spins) {
            const bool ok = !active || (__hip_atomic_load(myflag, RLX_AGENT) >= target);
            if (__all(ok)) break;
            __builtin_amdgcn_s_sleep(8);
            if ((spins & 63u) == 63u) {
                if (spins > kSpinCap) { __hip_atomic_store(tmo, 1u, RLX_AGENT); break; }
                if (__hip_atomic_load(tmo, RLX_AGENT) != 0u) break;
            }
        }
        __builtin_amdgcn_fence(__ATOMIC_ACQUIRE, "agent");
        asm volatile("s_waitcnt vmcnt(0)" ::: "memory");
    }
    __syncthreads();
}
__device__ __forceinline__ void signal_flag(gu32* flag) {
    asm volatile("s_waitcnt vmcnt(0)" ::: "memory");
    __syncthreads();
    if (threadIdx.x == 0) __hip_atomic_fetch_add(flag, 1u, RLX_AGENT);
}

__device__ __forceinline__ void transpose_tile(const u16* __restrict__ S, int lds_, u16* D, int ldd, int k0, int n0, unsigned char* lds, unsigned char* ws) {
    const int tid = opaque_tid();
    u16* T = (u16*)lds;
#pragma unroll
    for (int i = 0; i < 2; ++i) {
        const int c = tid + 256 * i, row = c >> 3, ch = c & 7;
        const v4u v = *(const v4u*)(S + (size_t)(k0 + row) * lds_ + n0 + ch * 8);
        const unsigned wv[4] = {v.x, v.y, v.z, v.w};
#pragma unroll
        for (int e = 0; e < 4; ++e) {
            T[(ch * 8 + 2 * e) * 72 + row] = (u16)wv[e];
            T[(ch * 8 + 2 * e + 1) * 72 + row] = (u16)(wv[e] >> 16);
        }
    }
    __syncthreads();
    const rsrc_t dr = __builtin_amdgcn_make_buffer_rsrc((void*)D, 0, DM * ldd * 2, 0x00020000);
#pragma unroll
    for (int i = 0; i < 2; ++i) {
        const int c = tid + 256 * i, n = c >> 3, ch = c & 7;
        const v4u v = *(const v4u*)(T + n * 72 + ch * 8);
        st16(dr, ((n0 + n) * ldd + k0 + ch * 8) * 2, v);
    }
}

__device__ __forceinline__ void attn_x(const Args& a, int s, int h) {
    const int lane = opaque_tid() & 63, c16 = lane & 15, g = lane >> 4;
    const u16* Q = (const u16*)(a.ws + Q_OFF); const u16* K = (const u16*)(a.ws + K_OFF); const u16* Vt = (const u16*)(a.ws + VT_OFF);
    const u16* Km = (const u16*)(a.ws + KM_OFF); const u16* Vm = (const u16*)(a.ws + VM_OFF); u16* O = (u16*)(a.ws + O_OFF);
    const float scale = 0.08838834764831845f;
    bf16x8 qf[4][2], kf[4][3], vxf[8], vmf[8];
#pragma unroll
    for (int ks = 0; ks < 4; ++ks) {
        const int eo = h * DH + 32 * ks + 8 * g;
        qf[ks][0] = *(const bf16x8*)(Q + (size_t)(s * SEG + c16) * HD + eo);
        qf[ks][1] = *(const bf16x8*)(Q + (size_t)(s * SEG + 16 + c16) * HD + eo);
        kf[ks][0] = *(const bf16x8*)(Km + (size_t)(s * MEMN + (c16 & 7)) * HD + eo);
        kf[ks][1] = *(const bf16x8*)(K + (size_t)(s * SEG + c16) * HD + eo);
        kf[ks][2] = *(const bf16x8*)(K + (size_t)(s * SEG + 16 + c16) * HD + eo);
    }
#pragma unroll
    for (int et = 0; et < 8; ++et) {
        const int e = h * DH + et * 16 + c16;
        vxf[et] = *(const bf16x8*)(Vt + ((size_t)s * HD + e) * 32 + 8 * g);
#pragma unroll
        for (int r = 0; r < 4; ++r) { vmf[et][r] = (short)Vm[(size_t)(s * MEMN + 4 * (g & 1) + r) * HD + e]; vmf[et][4 + r] = 0; }
    }
    __builtin_amdgcn_sched_barrier(0);
    f32x4 st[3][2];
#pragma unroll
    for (int t = 0; t < 3; ++t) { st[t][0] = (f32x4){0.f, 0.f, 0.f, 0.f}; st[t][1] = (f32x4){0.f, 0.f, 0.f, 0.f}; }
#pragma unroll
    for (int ks = 0; ks < 4; ++ks)
#pragma unroll
        for (int t = 0; t < 3; ++t) { st[t][0] = mfma32(kf[ks][t], qf[ks][0], st[t][0]); st[t][1] = mfma32(kf[ks][t], qf[ks][1], st[t][1]); }
    bf16x8 px[2], pm[2];
#pragma unroll
    for (int qt = 0; qt < 2; ++qt) {
        const int qi = qt * 16 + c16;
        float x[3][4]; float mx = -INFINITY;
#pragma unroll
        for (int t = 0; t < 3; ++t)
#pragma unroll
            for (int r = 0; r < 4; ++r) {
                const float sv = st[t][qt][r];
                float v = rbf(rbf(sv) * scale);
                const int key = (t - 1) * 16 + 4 * g + r;
                const bool valid = (t == 0) ? (g < 2) : (key <= qi);
                if (!valid) v = -INFINITY;
                x[t][r] = v; mx = fmaxf(mx, v);
            }
        mx = xmax16_32(mx);
        float sum = 0.f;
#pragma unroll
        for (int t = 0; t < 3; ++t)
#pragma unroll
            for (int r = 0; r < 4; ++r) { x[t][r] = __expf(x[t][r] - mx); sum += x[t][r]; }
        sum = xsum16_32(sum);
        const float inv = 1.0f / sum;
#pragma unroll
        for (int r = 0; r < 4; ++r) {
            pm[qt][r] = (short)f2bf(x[0][r] * inv); pm[qt][4 + r] = 0;
            px[qt][r] = (short)f2bf(x[1][r] * inv);
            px[qt][4 + r] = (short)f2bf(x[2][r] * inv);
        }
    }
#pragma unroll
    for (int et = 0; et < 8; ++et) {
        const bf16x8 vx = vxf[et], vm = vmf[et];
#pragma unroll
        for (int qt = 0; qt < 2; ++qt) {
            f32x4 o = mfma32(vx, px[qt], (f32x4){0.f, 0.f, 0.f, 0.f});
            o = mfma32(vm, pm[qt], o);
            const float o0 = o[0], o1 = o[1], o2 = o[2], o3 = o[3];
            v2u pk;
            pk.x = (unsigned)f2bf(o0) | ((unsigned)f2bf(o1) << 16);
            pk.y = (unsigned)f2bf(o2) | ((unsigned)f2bf(o3) << 16);
            st8(O + (size_t)(s * SEG + qt * 16 + c16) * HD + h * DH + et * 16 + 4 * g, pk);
        }
    }
}

__device__ __forceinline__ void oproj_task(const Args& a, int s, int ns, unsigned char* lds) {
    const int tid = opaque_tid(), w = tid >> 6, lane = tid & 63, c16 = lane & 15, g = lane >> 4;
    const u16* O = (const u16*)(a.ws + O_OFF) + (size_t)(s * SEG + c16) * HD + w * 1024 + 8 * g;
    const u16* Wt = (const u16*)(a.ws + WOT_OFF) + (size_t)(ns * 64 + c16) * HD + w * 1024 + 8 * g;
    f32x4 acc[4][2];
#pragma unroll
    for (int nt = 0; nt < 4; ++nt) { acc[nt][0] = (f32x4){0.f, 0.f, 0.f, 0.f}; acc[nt][1] = (f32x4){0.f, 0.f, 0.f, 0.f}; }
#pragma unroll 1
    for (int kb = 0; kb < 4; ++kb) {
        bf16x8 of[8][2], wf[8][4];
#pragma unroll
        for (int k8 = 0; k8 < 8; ++k8) {
            const int ko = (kb * 8 + k8) * 32;
            of[k8][0] = *(const bf16x8*)(O + ko); of[k8][1] = *(const bf16x8*)(O + (size_t)16 * HD + ko);
#pragma unroll
            for (int nt = 0; nt < 4; ++nt) wf[k8][nt] = *(const bf16x8*)(Wt + (size_t)nt * 16 * HD + ko);
        }
        __builtin_amdgcn_sched_barrier(0);
#pragma unroll
        for (int k8 = 0; k8 < 8; ++k8)
#pragma unroll
            for (int nt = 0; nt < 4; ++nt) {
                acc[nt][0] = mfma32(wf[k8][nt], of[k8][0], acc[nt][0]);
                acc[nt][1] = mfma32(wf[k8][nt], of[k8][1], acc[nt][1]);
            }
    }
    f32x4* R = (f32x4*)lds;
#pragma unroll
    for (int nt = 0; nt < 4; ++nt) { R[(w * 8 + nt * 2) * 64 + lane] = acc[nt][0]; R[(w * 8 + nt * 2 + 1) * 64 + lane] = acc[nt][1]; }
    __syncthreads();
#pragma unroll
    for (int i = 0; i < 2; ++i) {
        const int tile = w * 2 + i, nt = tile >> 1, mt = tile & 1;
        const f32x4 v = (R[tile * 64 + lane] + R[(8 + tile) * 64 + lane]) + (R[(16 + tile) * 64 + lane] + R[(24 + tile) * 64 + lane]);
        const float v0 = v[0], v1 = v[1], v2 = v[2], v3 = v[3];
        v2u pk;
        pk.x = (unsigned)f2bf(v0) | ((unsigned)f2bf(v1) << 16);
        pk.y = (unsigned)f2bf(v2) | ((unsigned)f2bf(v3) << 16);
        *(v2u*)(a.out + (size_t)(s * SEG + mt * 16 + c16) * (2 * DM) + ns * 64 + nt * 16 + 4 * g) = pk;
    }
    __syncthreads();
}

__device__ __forceinline__ void oproj_fine_task(const Args& a, int s, int n16, unsigned char* lds, gu32* tmo) {
    const int tid = opaque_tid(), w = tid >> 6, lane = tid & 63, c16 = lane & 15, g = lane >> 4;
    const u16* O = (const u16*)(a.ws + O_OFF) + (size_t)(s * SEG + c16) * HD + w * 1024 + 8 * g;
    const u16* Wt = (const u16*)(a.ws + WOT_OFF) + (size_t)(n16 * 16 + c16) * HD + w * 1024 + 8 * g;
    wait_flags(flagp(a.ws, F_TR), 740u, tid == 0, tmo);
    bf16x8 wf[32];
#pragma unroll
    for (int ks = 0; ks < 32; ++ks) wf[ks] = *(const bf16x8*)(Wt + ks * 32);
    wait_flags(flagp(a.ws, F_ATT + s), 8u, tid == 0, tmo);
    f32x4 acc[2] = {(f32x4){0.f, 0.f, 0.f, 0.f}, (f32x4){0.f, 0.f, 0.f, 0.f}};
#pragma unroll
    for (int kb = 0; kb < 2; ++kb) {
        bf16x8 of[16][2];
#pragma unroll
        for (int k = 0; k < 16; ++k) { of[k][0] = *(const bf16x8*)(O + (kb * 16 + k) * 32); of[k][1] = *(const bf16x8*)(O + (size_t)16 * HD + (kb * 16 + k) * 32); }
        __builtin_amdgcn_sched_barrier(0);
#pragma unroll
        for (int k = 0; k < 16; ++k) { acc[0] = mfma32(wf[kb * 16 + k], of[k][0], acc[0]); acc[1] = mfma32(wf[kb * 16 + k], of[k][1], acc[1]); }
    }
    f32x4* R = (f32x4*)lds;
    R[(w * 2) * 64 + lane] = acc[0]; R[(w * 2 + 1) * 64 + lane] = acc[1];
    __syncthreads();
    if (w < 2) {
        const f32x4 v = (R[w * 64 + lane] + R[(2 + w) * 64 + lane]) + (R[(4 + w) * 64 + lane] + R[(6 + w) * 64 + lane]);
        const float v0 = v[0], v1 = v[1], v2 = v[2], v3 = v[3];
        v2u pk;
        pk.x = (unsigned)f2bf(v0) | ((unsigned)f2bf(v1) << 16);
        pk.y = (unsigned)f2bf(v2) | ((unsigned)f2bf(v3) << 16);
        *(v2u*)(a.out + (size_t)(s * SEG + w * 16 + c16) * (2 * DM) + n16 * 16 + 4 * g) = pk;
    }
    __syncthreads();
}

__device__ __forceinline__ void bfinal_task(const Args& a, int rb32, unsigned char* lds) {
    const int tid = opaque_tid(), w = tid >> 6, lane = tid & 63, c16 = lane & 15, g = lane >> 4;
    const u16* Bq = (const u16*)(a.ws + BQKV_OFF);
    constexpr int OST = 1296;
    const float scale = 0.22360679774997896f;
    for (int it = 0; it < 4; ++it) {
        const int idx = it * 256 + tid, h = idx & 31, row = idx >> 5, pos = rb32 * 32 + row;
        const u16* qp = Bq + (size_t)pos * BD3 + h * BDH;
        float q[BDH];
#pragma unroll
        for (int d = 0; d < BDH; d += 2) { const unsigned u = *(const unsigned*)(qp + d); q[d] = bf2f((u16)u); q[d + 1] = bf2f((u16)(u >> 16)); }
        float lg[8]; float mx = -INFINITY;
#pragma unroll
        for (int wi = 0; wi < 8; ++wi) {
            const int kp = pos - 2 * wi;
            float v = -INFINITY;
            if (kp >= 0) {
                const u16* kpn = Bq + (size_t)kp * BD3 + DM + h * BDH;
                float d0 = 0.f;
#pragma unroll
                for (int d = 0; d < BDH; d += 2) { const unsigned u = *(const unsigned*)(kpn + d); d0 += q[d] * bf2f((u16)u); d0 += q[d + 1] * bf2f((u16)(u >> 16)); }
                v = rbf(rbf(d0) * scale);
            }
            lg[wi] = v; mx = fmaxf(mx, v);
        }
        float sum = 0.f;
#pragma unroll
        for (int wi = 0; wi < 8; ++wi) { lg[wi] = __expf(lg[wi] - mx); sum += lg[wi]; }
        const float inv = 1.0f / sum;
        float o[BDH];
#pragma unroll
        for (int d = 0; d < BDH; ++d) o[d] = 0.f;
#pragma unroll
        for (int wi = 0; wi < 8; ++wi) {
            const int kp = pos - 2 * wi;
            if (kp >= 0) {
                const float p = rbf(lg[wi] * inv);
                const u16* vpn = Bq + (size_t)kp * BD3 + 2 * DM + h * BDH;
#pragma unroll
                for (int d = 0; d < BDH; d += 2) { const unsigned u = *(const unsigned*)(vpn + d); o[d] += p * bf2f((u16)u); o[d + 1] += p * bf2f((u16)(u >> 16)); }
            }
        }
#pragma unroll
        for (int d = 0; d < BDH; d += 2) *(unsigned*)(lds + row * OST + (h * BDH + d) * 2) = (unsigned)f2bf(o[d]) | ((unsigned)f2bf(o[d + 1]) << 16);
    }
    __syncthreads();
    const u16* Wt = (const u16*)(a.ws + BWOT_OFF) + (size_t)(w * 160 + c16) * DM + 8 * g;
#pragma unroll 1
    for (int nh = 0; nh < 2; ++nh) {
        f32x4 acc[5][2];
#pragma unroll
        for (int nt = 0; nt < 5; ++nt) { acc[nt][0] = (f32x4){0.f, 0.f, 0.f, 0.f}; acc[nt][1] = (f32x4){0.f, 0.f, 0.f, 0.f}; }
#pragma unroll 4
        for (int ks = 0; ks < 20; ++ks) {
            const bf16x8 o0 = *(const bf16x8*)(lds + c16 * OST + ks * 64 + g * 16), o1 = *(const bf16x8*)(lds + (16 + c16) * OST + ks * 64 + g * 16);
#pragma unroll
            for (int nt = 0; nt < 5; ++nt) {
                const bf16x8 wf = *(const bf16x8*)(Wt + (size_t)(nh * 5 + nt) * 16 * DM + ks * 32);
                acc[nt][0] = mfma32(wf, o0, acc[nt][0]);
                acc[nt][1] = mfma32(wf, o1, acc[nt][1]);
            }
        }
#pragma unroll
        for (int nt = 0; nt < 5; ++nt)
#pragma unroll
            for (int mt = 0; mt < 2; ++mt) {
                const float v0 = acc[nt][mt][0], v1 = acc[nt][mt][1], v2 = acc[nt][mt][2], v3 = acc[nt][mt][3];
                v2u pk;
                pk.x = (unsigned)f2bf(v0) | ((unsigned)f2bf(v1) << 16);
                pk.y = (unsigned)f2bf(v2) | ((unsigned)f2bf(v3) << 16);
                *(v2u*)(a.out + (size_t)(rb32 * 32 + mt * 16 + c16) * (2 * DM) + DM + w * 160 + (nh * 5 + nt) * 16 + 4 * g) = pk;
            }
    }
    __syncthreads();
}

constexpr int NWORK = 192;
constexpr int T_KV = 0, T_TR = T_KV + 1024, T_Q = T_TR + 740, T_BQ = T_Q + 512, T_BF = T_BQ + 240, T_SEG = T_BF + 64, T_LAST = T_SEG + 60 * 18, T_END = T_LAST + 4 * 48;

__device__ __forceinline__ void worker_role(const Args& a, unsigned char* lds) {
    const int wid = blockIdx.x - 64, tid = threadIdx.x, w = tid >> 6;
    gu32* tmo = (gu32*)(a.ws + TMO_OFF);
    u16* Q = (u16*)(a.ws + Q_OFF); u16* K = (u16*)(a.ws + K_OFF); u16* Vt = (u16*)(a.ws + VT_OFF); u16* Bq = (u16*)(a.ws + BQKV_OFF);
    for (int T = wid; T < T_END; T += NWORK) {
        if (T < T_TR) {
            const int rb = T >> 6, r = T & 63, hc = r & 31;
            if (r < 32) gemm_tile<0>(a.Ax, DM, a.Wk, HD, K, HD, DM, rb * 128, hc * 128, lds);
            else gemm_tile<1>(a.Ax, DM, a.Wv, HD, Vt, HD, DM, rb * 128, hc * 128, lds);
            signal_flag(flagp(a.ws, F_KV + rb * 32 + hc));
        } else if (T < T_Q) {
            const int t = T - T_TR;
            if (t < 640) transpose_tile(a.Wo, DM, (u16*)(a.ws + WOT_OFF), HD, (t / 10) * 64, (t % 10) * 64, lds, a.ws);
            else transpose_tile(a.Bwo, DM, (u16*)(a.ws + BWOT_OFF), DM, ((t - 640) / 10) * 64, ((t - 640) % 10) * 64, lds, a.ws);
            signal_flag(flagp(a.ws, F_TR));
        } else if (T < T_BQ) {
            const int t = T - T_Q, rb = t >> 5, hc = t & 31;
            gemm_tile<0>(a.Ax, DM, a.Wq, HD, Q, HD, DM, rb * 128, hc * 128, lds);
            signal_flag(flagp(a.ws, F_Q + rb * 32 + hc));
        } else if (T < T_BF) {
            const int t = T - T_BQ, rb = t / 15, nt = t - rb * 15;
            gemm_tile<0>(a.Bx, DM, a.Bw, BD3, Bq, BD3, DM, rb * 128, nt * 128, lds);
            signal_flag(flagp(a.ws, F_BQ + rb));
        } else if (T < T_SEG) {
            const int rb32 = T - T_BF, rb = rb32 >> 2;
            const int fi = tid == 0 ? F_BQ + rb : (tid == 1 ? F_BQ + (rb > 0 ? rb - 1 : 0) : F_TR);
            wait_flags(flagp(a.ws, fi), tid < 2 ? 15u : 740u, tid < 3, tmo);
            bfinal_task(a, rb32, lds);
        } else {
            const int t = T - T_SEG, s = T < T_LAST ? t / 18 : 60 + (T - T_LAST) / 48, r = T < T_LAST ? t - s * 18 : (T - T_LAST) % 48, rb = s >> 2;
            if (T >= T_LAST && r >= 8) {
                oproj_fine_task(a, s, r - 8, lds, tmo);
            } else if (r < 8) {
                const int hh = r * 4 + (tid & 3), kind = (tid >> 2) & 3;
                const int fi = kind == 0 ? F_KV + rb * 32 + hh : (kind == 1 ? F_Q + rb * 32 + hh : F_KM + s * 32 + hh);
                wait_flags(flagp(a.ws, fi), kind == 1 ? 1u : 2u, tid < 12, tmo);
                attn_x(a, s, r * 4 + w);
                signal_flag(flagp(a.ws, F_ATT + s));
            } else {
                wait_flags(flagp(a.ws, tid == 0 ? F_ATT + s : F_TR), tid == 0 ? 8u : 740u, tid < 2, tmo);
                oproj_task(a, s, r - 8, lds);
            }
        }
    }
}

__global__ __launch_bounds__(256, 1) void fused_kernel(Args a) {
    extern __shared__ __attribute__((aligned(16))) unsigned char lds[];
    if (blockIdx.x < 64) chain_role(a, lds);
    else worker_role(a, lds);
}

extern "C" void kernel_launch(void* const* d_in, const int* in_sizes, int n_in,
                              void* d_out, int out_size, void* d_ws, size_t ws_size,
                              hipStream_t stream) {
    const u16* Ax = (const u16*)d_in[0];
    const u16* mem0 = (const u16*)d_in[1];
    const u16* Wq = (const u16*)d_in[2];
    const u16* Wk = (const u16*)d_in[3];
    const u16* Wv = (const u16*)d_in[4];
    const u16* Wo = (const u16*)d_in[5];
    const u16* Bx = (const u16*)d_in[7];
    const u16* Bw = (const u16*)d_in[8];
    const u16* Bwo = (const u16*)d_in[9];
    unsigned char* ws = (unsigned char*)d_ws;
    u16* out = (u16*)d_out;

    (void)hipMemsetAsync(ws, 0, XCHG_BYTES, stream);
    Args a{Ax, mem0, Wq, Wk, Wv, Wo, Bx, Bw, Bwo, out, ws};
    (void)hipFuncSetAttribute((const void*)fused_kernel, hipFuncAttributeMaxDynamicSharedMemorySize, CHAIN_LDS);
    fused_kernel<<<256, 256, CHAIN_LDS, stream>>>(a);
}
```

```cpp
#include <hip/hip_runtime.h>
#include <hip/hip_bf16.h>
#include <stdint.h>

#define GAS __attribute__((address_space(1)))
#define LAS __attribute__((address_space(3)))
typedef unsigned short u16;
typedef short bf16x8 __attribute__((ext_vector_type(8)));
typedef short bf16x4 __attribute__((ext_vector_type(4)));
typedef float f32x4 __attribute__((ext_vector_type(4)));
typedef unsigned v4u __attribute__((ext_vector_type(4)));
typedef unsigned v2u __attribute__((ext_vector_type(2)));
typedef GAS unsigned long long gu64;
typedef GAS unsigned gu32;
typedef LAS bf16x4 lds_bf16x4;
#define RLX_AGENT __ATOMIC_RELAXED, __HIP_MEMORY_SCOPE_AGENT

constexpr int SEQ = 2048, DM = 640, NH = 32, DH = 128, MEMN = 8, SEG = 32, NSEG = 64, HD = 4096;
constexpr int BD3 = 1920, BDH = 20;

constexpr size_t XA_OFF = 0;
constexpr size_t XA_BYTES = (size_t)2 * 64 * 512 * 8;
constexpr size_t XB_OFF = XA_OFF + XA_BYTES;
constexpr size_t XB_BYTES = (size_t)2 * 64 * 2560 * 8;
constexpr size_t XC_OFF = XB_OFF + XB_BYTES;
constexpr int XC_REP = 1;
constexpr size_t XC_BYTES = (size_t)2 * XC_REP * 2560 * 8;
constexpr size_t TMO_OFF = XC_OFF + XC_BYTES;
constexpr size_t FL_OFF = TMO_OFF + 256;
constexpr int F_KV = 0, F_Q = F_KV + 512, F_KM = F_Q + 512, F_ATT = F_KM + 2048, F_BQ = F_ATT + 64, F_TR = F_BQ + 16, F_END = F_TR + 1;
constexpr size_t XCHG_BYTES = (FL_OFF + (size_t)F_END * 64 + 255) / 256 * 256;
constexpr size_t Q_OFF = (XCHG_BYTES + 4095) / 4096 * 4096;
constexpr size_t K_OFF = Q_OFF + (size_t)SEQ * HD * 2;
constexpr size_t VT_OFF = K_OFF + (size_t)SEQ * HD * 2;
constexpr size_t KM_OFF = VT_OFF + (size_t)SEQ * HD * 2;
constexpr size_t VM_OFF = KM_OFF + (size_t)NSEG * MEMN * HD * 2;
constexpr size_t O_OFF = VM_OFF + (size_t)NSEG * MEMN * HD * 2;
constexpr size_t BQKV_OFF = O_OFF + (size_t)SEQ * HD * 2;
constexpr size_t WOT_OFF = BQKV_OFF + (size_t)SEQ * BD3 * 2;
constexpr size_t BWOT_OFF = WOT_OFF + (size_t)DM * HD * 2;
constexpr size_t WS_END = BWOT_OFF + (size_t)DM * DM * 2;

__device__ __forceinline__ int opaque_tid() { int t = threadIdx.x; asm volatile("" : "+v"(t)); return t; }
__device__ __forceinline__ u16 f2bf(float f) { return __builtin_bit_cast(u16, (__bf16)f); }
__device__ __forceinline__ float bf2f(u16 b) { return __builtin_bit_cast(float, (unsigned)b << 16); }
__device__ __forceinline__ float rbf(float f) { return bf2f(f2bf(f)); }
__device__ __forceinline__ f32x4 mfma32(bf16x8 a, bf16x8 b, f32x4 c) { return __builtin_amdgcn_mfma_f32_16x16x32_bf16(a, b, c, 0, 0, 0); }

__device__ __forceinline__ void st8(u16* p, v2u v) { __hip_atomic_store((gu64*)p, (unsigned long long)v.x | ((unsigned long long)v.y << 32), RLX_AGENT); }
__device__ __forceinline__ unsigned off_b(unsigned row, unsigned ch) { return 256u * row + 16u * (ch ^ (((row & 3) << 2) | ((row >> 2) & 3))); }

template <int MODE>
__device__ __forceinline__ void gemm_tile(const u16* __restrict__ A, int lda, const u16* __restrict__ W, int ldw,
                                          u16* __restrict__ C, int ldc, int K, int m0, int n0, unsigned char* lds) {
    const int tid = opaque_tid(), w = tid >> 6, lane = tid & 63, c16 = lane & 15, g = lane >> 4;
    const int wm = w >> 1, wn = w & 1;
    unsigned char* LA = lds;
    unsigned char* LW = lds + 32768;
    f32x4 acc[4][4];
#pragma unroll
    for (int i = 0; i < 4; ++i)
#pragma unroll
        for (int jn = 0; jn < 4; ++jn) acc[i][jn] = (f32x4){0.f, 0.f, 0.f, 0.f};
    constexpr int NK = 10, RD = 4;
    v4u ra[RD][4], rw[RD][4];
    const u16* Ap = A + (size_t)(m0 + (tid >> 3)) * lda + (tid & 7) * 8;
    const u16* Wp = W + (size_t)(tid >> 4) * ldw + n0 + (tid & 15) * 8;
    const unsigned q4 = (lane & 15) >> 2, p4 = lane & 3;
#pragma unroll
    for (int kt = 0; kt < RD; ++kt)
#pragma unroll
        for (int i = 0; i < 4; ++i) {
            ra[kt][i] = *(const v4u*)(Ap + (size_t)i * 32 * lda + kt * 64);
            rw[kt][i] = *(const v4u*)(Wp + (size_t)(kt * 64 + i * 16) * ldw);
        }
#pragma unroll
    for (int kt = 0; kt < NK; ++kt) {
        const int buf = kt & 1, sl = kt % RD;
#pragma unroll
        for (int i = 0; i < 4; ++i) {
            const int c = tid + 256 * i;
            const int row = c >> 3, ch = c & 7;
            *(v4u*)(LA + buf * 16384 + row * 128 + 16 * (ch ^ (row & 7))) = ra[sl][i];
            const int r2 = c >> 4, ch2 = c & 15;
            *(v4u*)(LW + buf * 16384 + (r2 >> 5) * 8192 + off_b(r2 & 31, ch2)) = rw[sl][i];
        }
        __syncthreads();
        if (kt + RD < NK) {
#pragma unroll
            for (int i = 0; i < 4; ++i) {
                ra[sl][i] = *(const v4u*)(Ap + (size_t)i * 32 * lda + (kt + RD) * 64);
                rw[sl][i] = *(const v4u*)(Wp + (size_t)((kt + RD) * 64 + i * 16) * ldw);
            }
        }
#pragma unroll
        for (int ks = 0; ks < 2; ++ks) {
            bf16x8 af[4], wf[4];
#pragma unroll
            for (int mt = 0; mt < 4; ++mt) {
                const int row = wm * 64 + mt * 16 + c16, ch = ks * 4 + g;
                af[mt] = *(const bf16x8*)(LA + buf * 16384 + row * 128 + 16 * (ch ^ (row & 7)));
            }
#pragma unroll
            for (int nt = 0; nt < 4; ++nt) {
                const unsigned cb = wn * 4 + nt;
                unsigned char* base = LW + buf * 16384 + ks * 8192;
                const bf16x4 lo = __builtin_amdgcn_ds_read_tr16_b64_v4i16((lds_bf16x4*)(base + off_b(8 * g + q4, 2 * cb + (p4 >> 1)) + 8 * (p4 & 1)));
                const bf16x4 hi = __builtin_amdgcn_ds_read_tr16_b64_v4i16((lds_bf16x4*)(base + off_b(8 * g + 4 + q4, 2 * cb + (p4 >> 1)) + 8 * (p4 & 1)));
                wf[nt] = __builtin_shufflevector(lo, hi, 0, 1, 2, 3, 4, 5, 6, 7);
            }
#pragma unroll
            for (int mt = 0; mt < 4; ++mt)
#pragma unroll
                for (int nt = 0; nt < 4; ++nt)
                    acc[mt][nt] = (MODE == 0) ? mfma32(wf[nt], af[mt], acc[mt][nt]) : mfma32(af[mt], wf[nt], acc[mt][nt]);
        }
    }
    __syncthreads();
#pragma unroll
    for (int mt = 0; mt < 4; ++mt)
#pragma unroll
        for (int nt = 0; nt < 4; ++nt) {
            v2u pk;
            pk.x = (unsigned)f2bf(acc[mt][nt][0]) | ((unsigned)f2bf(acc[mt][nt][1]) << 16);
            pk.y = (unsigned)f2bf(acc[mt][nt][2]) | ((unsigned)f2bf(acc[mt][nt][3]) << 16);
            if (MODE == 0) {
                const int m = m0 + wm * 64 + mt * 16 + c16, n = n0 + wn * 64 + nt * 16 + 4 * g;
                st8(C + (size_t)m * ldc + n, pk);
            } else {
                const int m = m0 + wm * 64 + mt * 16 + 4 * g, n = n0 + wn * 64 + nt * 16 + c16;
                st8(C + ((size_t)(m >> 5) * HD + n) * 32 + 8 * g + 4 * ((m >> 4) & 1), pk);
            }
        }
}

struct Args { const u16* Ax; const u16* mem0; const u16* Wq; const u16* Wk; const u16* Wv; const u16* Wo;
              const u16* Bx; const u16* Bw; const u16* Bwo; u16* out; unsigned char* ws; };

constexpr unsigned kSpinCap = 1u << 17;
#define SLEEP_A 0
#define SLEEP_B 16
#define SLEEP_C 16

typedef __amdgpu_buffer_rsrc_t rsrc_t;
__device__ __forceinline__ void st16(rsrc_t rs, int off, v4u v) { __builtin_amdgcn_raw_buffer_store_b128(v, rs, off, 0, 16); }
template <int NPT, bool TAG16>
__device__ __forceinline__ void gather(rsrc_t rs, const int (&off)[NPT], unsigned tag, v4u (&v)[NPT], gu32* tmo) {
    for (unsigned spins = 0;; ++spins) {
        asm volatile("" ::: "memory");
#pragma unroll
        for (int k = 0; k < NPT; ++k) v[k] = __builtin_amdgcn_raw_buffer_load_b128(rs, off[k], 0, 16);
        bool ok = true;
#pragma unroll
        for (int k = 0; k < NPT; ++k) {
            if (TAG16) ok &= ((v[k].y >> 16) == tag) & ((v[k].w >> 16) == tag);
            else ok &= (v[k].y == tag) & (v[k].w == tag);
        }
        if (__all(ok)) break;
        if ((spins & 63u) == 63u) {
            if (spins > kSpinCap) { __hip_atomic_store(tmo, 1u, RLX_AGENT); break; }
            if (__hip_atomic_load(tmo, RLX_AGENT) != 0u) break;
        }
    }
}
__device__ __forceinline__ float xmax16_32(float v) {
    auto r = __builtin_amdgcn_permlane16_swap(__float_as_uint(v), __float_as_uint(v), false, false);
    v = fmaxf(__uint_as_float(r[0]), __uint_as_float(r[1]));
    auto r2 = __builtin_amdgcn_permlane32_swap(__float_as_uint(v), __float_as_uint(v), false, false);
    return fmaxf(__uint_as_float(r2[0]), __uint_as_float(r2[1]));
}
__device__ __forceinline__ float xsum16_32(float v) {
    auto r = __builtin_amdgcn_permlane16_swap(__float_as_uint(v), __float_as_uint(v), false, false);
    v = __uint_as_float(r[0]) + __uint_as_float(r[1]);
    auto r2 = __builtin_amdgcn_permlane32_swap(__float_as_uint(v), __float_as_uint(v), false, false);
    return __uint_as_float(r2[0]) + __uint_as_float(r2[1]);
}

constexpr int MEM_STRIDE = 1312, QK_STRIDE = 288;
constexpr int L_WO = 0;
constexpr int L_MEM = L_WO + 81920, L_Q = L_MEM + 8 * MEM_STRIDE, L_K = L_Q + 8 * QK_STRIDE, L_VT = L_K + 8 * QK_STRIDE,
              L_RED = L_VT + 128 * 32, L_KV = L_RED + 6 * 80 * 4, L_END = L_KV + 16 * 1024;
constexpr int L_STAGE = L_MEM;
constexpr int CHAIN_LDS = (L_STAGE + 51200 > L_END) ? (L_STAGE + 51200) : L_END;

__device__ __forceinline__ gu32* flagp(unsigned char* ws, int idx) { return (gu32*)(ws + FL_OFF + (size_t)idx * 64); }

__device__ __forceinline__ void chain_role(const Args& a, unsigned char* lds) {
    const int b_ = blockIdx.x, cu = ((b_ & 7) + 8 * (b_ >> 4)) * 2 + ((b_ >> 3) & 1), h = cu >> 1, j = cu & 1;
    const int tid = threadIdx.x, w = tid >> 6, lane = tid & 63, c16 = lane & 15, g = lane >> 4;
    gu32* tmo = (gu32*)(a.ws + TMO_OFF);
    const rsrc_t xr = __builtin_amdgcn_make_buffer_rsrc((void*)a.ws, 0, (int)TMO_OFF, 0x00020000);
    const rsrc_t kr_ = __builtin_amdgcn_make_buffer_rsrc((void*)(a.ws + K_OFF), 0, SEQ * HD * 2, 0x00020000);
    const rsrc_t vr_ = __builtin_amdgcn_make_buffer_rsrc((void*)(a.ws + VT_OFF), 0, SEQ * HD * 2, 0x00020000);
    u16* Km = (u16*)(a.ws + KM_OFF);
    u16* Vm = (u16*)(a.ws + VM_OFF);

    bf16x8 wqkv[3][20];
    {
        unsigned char* stg = lds + L_STAGE;
        const u16* Wqk = j ? a.Wk : a.Wq;
        const unsigned q4 = (lane & 15) >> 2, p4 = lane & 3;
        v4u tmp[2][12];
        auto issue = [&](int rd, v4u (&t)[12]) {
            if (rd < 5) {
#pragma unroll
                for (int i = 0; i < 12; ++i) {
                    const int c = tid + 256 * i;
                    const int row = c / 24, ch = c - row * 24;
                    const size_t kr = (size_t)(rd * 128 + row) * HD + h * DH;
                    t[i] = (ch < 16) ? *(const v4u*)(Wqk + kr + ch * 8) : *(const v4u*)(a.Wv + kr + j * 64 + (ch - 16) * 8);
                }
            } else {
#pragma unroll
                for (int i = 0; i < 10; ++i) {
                    const int c = tid + 256 * i;
                    const int row = c / 40, ch = c - row * 40;
                    t[i] = *(const v4u*)(a.Wo + (size_t)(h * DH + (rd - 5) * 64 + row) * DM + j * 320 + ch * 8);
                }
            }
        };
        issue(0, tmp[0]);
#pragma unroll
        for (int rd = 0; rd < 7; ++rd) {
            if (rd + 1 < 7) issue(rd + 1, tmp[(rd + 1) & 1]);
            if (rd < 5) {
#pragma unroll
                for (int i = 0; i < 12; ++i) {
                    const int c = tid + 256 * i;
                    const int row = c / 24, ch = c - row * 24;
                    *(v4u*)(stg + row * 400 + ch * 16) = tmp[rd & 1][i];
                }
            } else {
#pragma unroll
                for (int i = 0; i < 10; ++i) {
                    const int c = tid + 256 * i;
                    const int row = c / 40, ch = c - row * 40;
                    *(v4u*)(stg + row * 656 + ch * 16) = tmp[rd & 1][i];
                }
            }
            asm volatile("s_waitcnt lgkmcnt(0)" ::: "memory");
            __builtin_amdgcn_s_barrier();
            if (rd < 5) {
#pragma unroll
                for (int k4 = 0; k4 < 4; ++k4)
#pragma unroll
                    for (int t = 0; t < 3; ++t) {
                        const int col = (t < 2) ? ((2 * w + t) * 16) : (128 + w * 16);
                        unsigned char* p0 = stg + (k4 * 32 + 8 * g + q4) * 400 + (col + 4 * p4) * 2;
                        const bf16x4 lo = __builtin_amdgcn_ds_read_tr16_b64_v4i16((lds_bf16x4*)p0);
                        const bf16x4 hi = __builtin_amdgcn_ds_read_tr16_b64_v4i16((lds_bf16x4*)(p0 + 4 * 400));
                        wqkv[t][(rd < 5 ? rd : 0) * 4 + k4] = __builtin_shufflevector(lo, hi, 0, 1, 2, 3, 4, 5, 6, 7);
                    }
            } else {
#pragma unroll
                for (int k2 = 0; k2 < 2; ++k2)
#pragma unroll
                    for (int nt = 0; nt < 5; ++nt) {
                        unsigned char* p0 = stg + (k2 * 32 + 4 * g + q4) * 656 + (w * 80 + nt * 16 + 4 * p4) * 2;
                        const bf16x4 lo = __builtin_amdgcn_ds_read_tr16_b64_v4i16((lds_bf16x4*)p0);
                        const bf16x4 hi = __builtin_amdgcn_ds_read_tr16_b64_v4i16((lds_bf16x4*)(p0 + 16 * 656));
                        *(bf16x8*)(lds + L_WO + ((w * 5 + nt) * 4 + (rd - 5) * 2 + k2) * 1024 + lane * 16) = __builtin_shufflevector(lo, hi, 0, 1, 2, 3, 4, 5, 6, 7);
                    }
            }
            asm volatile("s_waitcnt lgkmcnt(0)" ::: "memory");
            __builtin_amdgcn_s_barrier();
        }
        if (tid < 128) *(v4u*)(lds + L_VT + tid * 32 + 16) = (v4u){0u, 0u, 0u, 0u};
    }
    const float scale = 0.08838834764831845f;
    const int tt = tid < 240 ? tid : 239, hg = tt / 40, pr = tt - hg * 40;

    for (int s = 0; s < NSEG; ++s) {
        const unsigned tag = (unsigned)(s + 1);
        const int par = s & 1;
        if ((s & 3) == 0) {
            gu32* fl = flagp(a.ws, F_KV + (s >> 2) * 32 + h);
            for (unsigned spins = 0;; ++spins) {
                if (__builtin_amdgcn_readfirstlane(__hip_atomic_load(fl, RLX_AGENT)) >= 2u) break;
                __builtin_amdgcn_s_sleep(4);
                if ((spins & 63u) == 63u) {
                    if (spins > kSpinCap) { __hip_atomic_store(tmo, 1u, RLX_AGENT); break; }
                    if (__hip_atomic_load(tmo, RLX_AGENT) != 0u) break;
                }
            }
            __builtin_amdgcn_fence(__ATOMIC_ACQUIRE, "wavefront");
        }
        v4u kvq[4];
#pragma unroll
        for (int i = 0; i < 4; ++i) {
            if (w < 2) kvq[i] = __builtin_amdgcn_raw_buffer_load_b128(kr_, ((s * SEG + w * 16 + c16) * HD + h * DH + 32 * i + 8 * g) * 2, 0, 16);
            else kvq[i] = __builtin_amdgcn_raw_buffer_load_b128(vr_, ((s * HD + h * DH + ((w - 2) * 4 + i) * 16 + c16) * 32 + 8 * g) * 2, 0, 16);
        }
        if (s == 0) {
            for (int i = tid; i < 8 * 80; i += 256) {
                const int tk = i / 80, ch = i % 80;
                *(v4u*)(lds + L_MEM + tk * MEM_STRIDE + ch * 16) = *(const v4u*)(a.mem0 + tk * DM + ch * 8);
            }
        } else {
            v4u v[5];
            __builtin_amdgcn_s_sleep(SLEEP_C);
            const int o0 = (int)XC_OFF + (((s - 1) & 1) * XC_REP + (cu & (XC_REP - 1))) * 20480 + tid * 16;
            const int offs[5] = {o0, o0 + 4096, o0 + 8192, o0 + 12288, o0 + 16384};
            gather<5, false>(xr, offs, (unsigned)s, v, tmo);
            {
                const int l2 = (2 * tid) & 63;
                const int tok = l2 & 7, col = ((tid >> 5) * 16 + (l2 >> 4) * 4 + ((l2 >> 3) & 1) * 2) * 2;
#pragma unroll
                for (int k = 0; k < 5; ++k) {
                    *(unsigned*)(lds + L_MEM + tok * MEM_STRIDE + col + k * 256) = v[k].x;
                    *(unsigned*)(lds + L_MEM + (tok + 1) * MEM_STRIDE + col + k * 256) = v[k].z;
                }
            }
        }
#pragma unroll
        for (int i = 0; i < 4; ++i) *(v4u*)(lds + L_KV + (w * 4 + i) * 1024 + lane * 16) = kvq[i];
        __syncthreads();
        f32x4 acc[3];
        {
            bf16x8 mb[20];
#pragma unroll
            for (int ks = 0; ks < 20; ++ks) mb[ks] = *(const bf16x8*)(lds + L_MEM + (c16 & 7) * MEM_STRIDE + ks * 64 + g * 16);
            __builtin_amdgcn_sched_barrier(0);
#pragma unroll
            for (int t = 0; t < 3; ++t) acc[t] = (f32x4){0.f, 0.f, 0.f, 0.f};
#pragma unroll
            for (int ks = 0; ks < 20; ++ks)
#pragma unroll
                for (int t = 0; t < 3; ++t) acc[t] = mfma32(wqkv[t][ks], mb[ks], acc[t]);
        }
        u16 qv[3][4];
#pragma unroll
        for (int t = 0; t < 3; ++t)
#pragma unroll
            for (int r = 0; r < 4; ++r) { const float f = acc[t][r]; qv[t][r] = f2bf(f); }
        if (s + 1 < NSEG) {
            const bool hi = c16 >= 8;
            v4u pk;
            pk.x = (unsigned)(hi ? qv[0][2] : qv[0][0]) | ((unsigned)(hi ? qv[1][2] : qv[1][0]) << 16);
            pk.y = (unsigned)(hi ? qv[2][2] : qv[2][0]) | (tag << 16);
            pk.z = (unsigned)(hi ? qv[0][3] : qv[0][1]) | ((unsigned)(hi ? qv[1][3] : qv[1][1]) << 16);
            pk.w = (unsigned)(hi ? qv[2][3] : qv[2][1]) | (tag << 16);
            st16(xr, (int)XA_OFF + (par * 64 + cu) * 4096 + tid * 16, pk);
        }
        if (c16 < 8) {
            const int token = c16;
            unsigned char* qk = lds + (j ? L_K : L_Q) + token * QK_STRIDE;
#pragma unroll
            for (int t = 0; t < 2; ++t) {
                v2u pk; pk.x = (unsigned)qv[t][0] | ((unsigned)qv[t][1] << 16); pk.y = (unsigned)qv[t][2] | ((unsigned)qv[t][3] << 16);
                *(v2u*)(qk + ((2 * w + t) * 16 + 4 * g) * 2) = pk;
            }
#pragma unroll
            for (int r = 0; r < 4; ++r) {
                const int e = j * 64 + w * 16 + 4 * g + r;
                *(u16*)(lds + L_VT + e * 32 + token * 2) = qv[2][r];
            }
        }
        auto store_kv = [&]() {
            if (c16 < 8) {
                if (j) {
#pragma unroll
                    for (int t = 0; t < 2; ++t) {
                        v2u pk; pk.x = (unsigned)qv[t][0] | ((unsigned)qv[t][1] << 16); pk.y = (unsigned)qv[t][2] | ((unsigned)qv[t][3] << 16);
                        st8(Km + (size_t)(s * MEMN + c16) * HD + h * DH + (2 * w + t) * 16 + 4 * g, pk);
                    }
                }
                v2u pk; pk.x = (unsigned)qv[2][0] | ((unsigned)qv[2][1] << 16); pk.y = (unsigned)qv[2][2] | ((unsigned)qv[2][3] << 16);
                st8(Vm + (size_t)(s * MEMN + c16) * HD + h * DH + j * 64 + w * 16 + 4 * g, pk);
            }
        };
        if (s + 1 == NSEG) {
            store_kv();
            asm volatile("s_waitcnt vmcnt(0)" ::: "memory");
            __syncthreads();
            if (tid == 0) __hip_atomic_fetch_add(flagp(a.ws, F_KM + s * 32 + h), 1u, RLX_AGENT);
        }
        if (s + 1 == NSEG) break;
        bf16x8 kx[2][4], vx[8];
#pragma unroll
        for (int f8 = 0; f8 < 8; ++f8) kx[f8 >> 2][f8 & 3] = *(const bf16x8*)(lds + L_KV + f8 * 1024 + lane * 16);
#pragma unroll
        for (int f8 = 0; f8 < 8; ++f8) vx[f8] = *(const bf16x8*)(lds + L_KV + (8 + f8) * 1024 + lane * 16);
        {
            v4u v[1];
            __builtin_amdgcn_s_sleep(SLEEP_A);
            const int offs[1] = {(int)XA_OFF + (par * 64 + (cu ^ 1)) * 4096 + tid * 16};
            gather<1, true>(xr, offs, tag, v, tmo);
            const int pj = j ^ 1;
            const int pw = tid >> 6, pg = (tid >> 4) & 3, token = tid & 7, r = ((tid >> 3) & 1) * 2;
            unsigned char* qk = lds + (pj ? L_K : L_Q) + token * QK_STRIDE;
            *(unsigned*)(qk + ((2 * pw) * 16 + 4 * pg + r) * 2) = (v[0].x & 0xffffu) | (v[0].z << 16);
            *(unsigned*)(qk + ((2 * pw + 1) * 16 + 4 * pg + r) * 2) = (v[0].x >> 16) | (v[0].z & 0xffff0000u);
            *(u16*)(lds + L_VT + (pj * 64 + pw * 16 + 4 * pg + r) * 32 + token * 2) = (u16)v[0].y;
            *(u16*)(lds + L_VT + (pj * 64 + pw * 16 + 4 * pg + r + 1) * 32 + token * 2) = (u16)v[0].w;
        }
        __syncthreads();
        store_kv();
        bf16x8 qf[4], kf[4];
        bf16x4 vm4[8];
#pragma unroll
        for (int ks = 0; ks < 4; ++ks) {
            qf[ks] = *(const bf16x8*)(lds + L_Q + (c16 & 7) * QK_STRIDE + ks * 64 + g * 16);
            kf[ks] = *(const bf16x8*)(lds + L_K + (c16 & 7) * QK_STRIDE + ks * 64 + g * 16);
        }
#pragma unroll
        for (int et = 0; et < 8; ++et) vm4[et] = *(const bf16x4*)(lds + L_VT + (et * 16 + c16) * 32 + g * 8);
        __builtin_amdgcn_sched_barrier(0);
        f32x4 st[3];
#pragma unroll
        for (int t = 0; t < 3; ++t) st[t] = (f32x4){0.f, 0.f, 0.f, 0.f};
#pragma unroll
        for (int ks = 0; ks < 4; ++ks) {
            st[1] = mfma32(kx[0][ks], qf[ks], st[1]);
            st[2] = mfma32(kx[1][ks], qf[ks], st[2]);
            st[0] = mfma32(kf[ks], qf[ks], st[0]);
        }
        __builtin_amdgcn_sched_barrier(0);
        bf16x8 wo[5][4];
#pragma unroll
        for (int nt = 0; nt < 5; ++nt)
#pragma unroll
            for (int ks = 0; ks < 4; ++ks) wo[nt][ks] = *(const bf16x8*)(lds + L_WO + ((w * 5 + nt) * 4 + ks) * 1024 + lane * 16);
        __builtin_amdgcn_sched_barrier(0);
        float x[3][4];
        float mx = -INFINITY;
#pragma unroll
        for (int t = 0; t < 3; ++t)
#pragma unroll
            for (int r = 0; r < 4; ++r) {
                const float sv = st[t][r];
                float v = rbf(rbf(sv) * scale);
                if (t == 0 && g >= 2) v = -INFINITY;
                x[t][r] = v; mx = fmaxf(mx, v);
            }
        mx = xmax16_32(mx);
        float sum = 0.f;
#pragma unroll
        for (int t = 0; t < 3; ++t)
#pragma unroll
            for (int r = 0; r < 4; ++r) { x[t][r] = __expf(x[t][r] - mx); sum += x[t][r]; }
        sum = xsum16_32(sum);
        const float inv = __builtin_amdgcn_rcpf(sum);
        bf16x8 px, pm;
#pragma unroll
        for (int r = 0; r < 4; ++r) {
            pm[r] = (short)f2bf(x[0][r] * inv); pm[4 + r] = 0;
            px[r] = (short)f2bf(x[1][r] * inv);
            px[4 + r] = (short)f2bf(x[2][r] * inv);
        }
        f32x4 oa[8];
#pragma unroll
        for (int et = 0; et < 8; ++et) oa[et] = mfma32(vx[et], px, (f32x4){0.f, 0.f, 0.f, 0.f});
#pragma unroll
        for (int et = 0; et < 8; ++et) {
            const bf16x8 vm = __builtin_shufflevector(vm4[et], (bf16x4){0, 0, 0, 0}, 0, 1, 2, 3, 4, 5, 6, 7);
            oa[et] = mfma32(vm, pm, oa[et]);
        }
        bf16x8 of[4];
#pragma unroll
        for (int et = 0; et < 8; ++et)
#pragma unroll
            for (int r = 0; r < 4; ++r) { const float f = oa[et][r]; of[et >> 1][(et & 1) * 4 + r] = (short)f2bf(f); }
        {
            f32x4 p[5];
#pragma unroll
            for (int nt = 0; nt < 5; ++nt) p[nt] = (f32x4){0.f, 0.f, 0.f, 0.f};
#pragma unroll
            for (int ks = 0; ks < 4; ++ks)
#pragma unroll
                for (int nt = 0; nt < 5; ++nt) p[nt] = mfma32(wo[nt][ks], of[ks], p[nt]);
            const int off = (int)XB_OFF + (par * 32 * 2560 + h * 2560 + (j * 4 + w) * 320 + lane) * 16;
            const bool hi = c16 >= 8;
#pragma unroll
            for (int nt = 0; nt < 5; ++nt) {
                const float p0 = p[nt][0], p1 = p[nt][1], p2 = p[nt][2], p3 = p[nt][3];
                v4u pk; pk.x = __float_as_uint(hi ? p2 : p0); pk.y = tag; pk.z = __float_as_uint(hi ? p3 : p1); pk.w = tag;
                st16(xr, off + nt * 1024, pk);
            }
        }
        {
            const int off = (int)XB_OFF + (par * 32 * 2560 + hg * 2560 + cu * 40 + pr) * 16;
            const int off5 = off + (hg < 2 ? 5 : 4) * 6 * 2560 * 16;
            v4u v[6];
            __builtin_amdgcn_s_sleep(SLEEP_B);
            constexpr int HS = 6 * 2560 * 16;
            const int offs[6] = {off, off + HS, off + 2 * HS, off + 3 * HS, off + 4 * HS, off5};
            gather<6, false>(xr, offs, tag, v, tmo);
            float s0 = 0.f, s1 = 0.f;
#pragma unroll
            for (int k = 0; k < 5; ++k) { s0 += __uint_as_float(v[k].x); s1 += __uint_as_float(v[k].z); }
            if (hg < 2) { s0 += __uint_as_float(v[5].x); s1 += __uint_as_float(v[5].z); }
            if (tid < 240) { v2u pk; pk.x = __float_as_uint(s0); pk.y = __float_as_uint(s1); *(v2u*)(lds + L_RED + tid * 8) = pk; }
        }
        asm volatile("s_waitcnt vmcnt(0)" ::: "memory");
        __syncthreads();
        if (tid == 192) __hip_atomic_fetch_add(flagp(a.ws, F_KM + s * 32 + h), 1u, RLX_AGENT);
        if (tid < 128) {
            const int t80 = tid < 80 ? tid : 79;
            const float* rp = (const float*)(lds + L_RED) + t80;
            const float sacc = ((rp[0] + rp[80]) + (rp[160] + rp[240])) + (rp[320] + rp[400]);
            const unsigned mine = f2bf(sacc);
            const unsigned m1 = (unsigned)__builtin_amdgcn_mov_dpp((int)mine, 0x55, 0xf, 0xf, true);
            const unsigned m2 = (unsigned)__builtin_amdgcn_mov_dpp((int)mine, 0xAA, 0xf, 0xf, true);
            const unsigned m3 = (unsigned)__builtin_amdgcn_mov_dpp((int)mine, 0xFF, 0xf, 0xf, true);
            if (tid < 80 && (tid & 3) == 0) {
                v4u pk; pk.x = mine | (m1 << 16); pk.y = tag; pk.z = m2 | (m3 << 16); pk.w = tag;
#pragma unroll
                for (int rp = 0; rp < XC_REP; ++rp) st16(xr, (int)XC_OFF + (par * XC_REP + rp) * 20480 + (cu * 20 + (tid >> 2)) * 16, pk);
            }
        }
    }
}

__device__ __forceinline__ void wait_flags(gu32* myflag, unsigned target, bool active, gu32* tmo) {
    if (threadIdx.x < 64) {
        for (unsigned spins = 0;; ++spins) {
            const bool ok = !active || (__hip_atomic_load(myflag, RLX_AGENT) >= target);
            if (__all(ok)) break;
            __builtin_amdgcn_s_sleep(8);
            if ((spins & 63u) == 63u) {
                if (spins > kSpinCap) { __hip_atomic_store(tmo, 1u, RLX_AGENT); break; }
                if (__hip_atomic_load(tmo, RLX_AGENT) != 0u) break;
            }
        }
        __builtin_amdgcn_fence(__ATOMIC_ACQUIRE, "agent");
        asm volatile("s_waitcnt vmcnt(0)" ::: "memory");
    }
    __syncthreads();
}
__device__ __forceinline__ void signal_flag(gu32* flag) {
    asm volatile("s_waitcnt vmcnt(0)" ::: "memory");
    __syncthreads();
    if (threadIdx.x == 0) __hip_atomic_fetch_add(flag, 1u, RLX_AGENT);
}

__device__ __forceinline__ void transpose_tile(const u16* __restrict__ S, int lds_, u16* D, int ldd, int k0, int n0, unsigned char* lds, unsigned char* ws) {
    const int tid = opaque_tid();
    u16* T = (u16*)lds;
#pragma unroll
    for (int i = 0; i < 2; ++i) {
        const int c = tid + 256 * i, row = c >> 3, ch = c & 7;
        const v4u v = *(const v4u*)(S + (size_t)(k0 + row) * lds_ + n0 + ch * 8);
        const unsigned wv[4] = {v.x, v.y, v.z, v.w};
#pragma unroll
        for (int e = 0; e < 4; ++e) {
            T[(ch * 8 + 2 * e) * 72 + row] = (u16)wv[e];
            T[(ch * 8 + 2 * e + 1) * 72 + row] = (u16)(wv[e] >> 16);
        }
    }
    __syncthreads();
    const rsrc_t dr = __builtin_amdgcn_make_buffer_rsrc((void*)D, 0, DM * ldd * 2, 0x00020000);
#pragma unroll
    for (int i = 0; i < 2; ++i) {
        const int c = tid + 256 * i, n = c >> 3, ch = c & 7;
        const v4u v = *(const v4u*)(T + n * 72 + ch * 8);
        st16(dr, ((n0 + n) * ldd + k0 + ch * 8) * 2, v);
    }
}

__device__ __forceinline__ void attn_x(const Args& a, int s, int h) {
    const int lane = opaque_tid() & 63, c16 = lane & 15, g = lane >> 4;
    const u16* Q = (const u16*)(a.ws + Q_OFF); const u16* K = (const u16*)(a.ws + K_OFF); const u16* Vt = (const u16*)(a.ws + VT_OFF);
    const u16* Km = (const u16*)(a.ws + KM_OFF); const u16* Vm = (const u16*)(a.ws + VM_OFF); u16* O = (u16*)(a.ws + O_OFF);
    const float scale = 0.08838834764831845f;
    bf16x8 qf[4][2], kf[4][3], vxf[8], vmf[8];
#pragma unroll
    for (int ks = 0; ks < 4; ++ks) {
        const int eo = h * DH + 32 * ks + 8 * g;
        qf[ks][0] = *(const bf16x8*)(Q + (size_t)(s * SEG + c16) * HD + eo);
        qf[ks][1] = *(const bf16x8*)(Q + (size_t)(s * SEG + 16 + c16) * HD + eo);
        kf[ks][1] = *(const bf16x8*)(K + (size_t)(s * SEG + c16) * HD + eo);
        kf[ks][2] = *(const bf16x8*)(K + (size_t)(s * SEG + 16 + c16) * HD + eo);
    }
#pragma unroll
    for (int et = 0; et < 8; ++et) vxf[et] = *(const bf16x8*)(Vt + ((size_t)s * HD + h * DH + et * 16 + c16) * 32 + 8 * g);
    {
        gu32* fl = flagp(a.ws, F_KM + s * 32 + h);
        gu32* tmo = (gu32*)(a.ws + TMO_OFF);
        for (unsigned spins = 0;; ++spins) {
            if (__builtin_amdgcn_readfirstlane(__hip_atomic_load(fl, RLX_AGENT)) >= 2u) break;
            __builtin_amdgcn_s_sleep(4);
            if ((spins & 63u) == 63u) {
                if (spins > 4 * kSpinCap) { __hip_atomic_store(tmo, 1u, RLX_AGENT); break; }
                if (__hip_atomic_load(tmo, RLX_AGENT) != 0u) break;
            }
        }
        __builtin_amdgcn_fence(__ATOMIC_ACQUIRE, "wavefront");
    }
    const rsrc_t kmr = __builtin_amdgcn_make_buffer_rsrc((void*)Km, 0, NSEG * MEMN * HD * 2, 0x00020000);
    const rsrc_t vmr = __builtin_amdgcn_make_buffer_rsrc((void*)Vm, 0, NSEG * MEMN * HD * 2, 0x00020000);
#pragma unroll
    for (int ks = 0; ks < 4; ++ks)
        kf[ks][0] = __builtin_bit_cast(bf16x8, __builtin_amdgcn_raw_buffer_load_b128(kmr, ((s * MEMN + (c16 & 7)) * HD + h * DH + 32 * ks + 8 * g) * 2, 0, 16));
#pragma unroll
    for (int et = 0; et < 8; ++et) {
#pragma unroll
        for (int r = 0; r < 4; ++r) {
            vmf[et][r] = (short)__builtin_amdgcn_raw_buffer_load_b16(vmr, ((s * MEMN + 4 * (g & 1) + r) * HD + h * DH + et * 16 + c16) * 2, 0, 16);
            vmf[et][4 + r] = 0;
        }
    }
    __builtin_amdgcn_sched_barrier(0);
    f32x4 st[3][2];
#pragma unroll
    for (int t = 0; t < 3; ++t) { st[t][0] = (f32x4){0.f, 0.f, 0.f, 0.f}; st[t][1] = (f32x4){0.f, 0.f, 0.f, 0.f}; }
#pragma unroll
    for (int ks = 0; ks < 4; ++ks)
#pragma unroll
        for (int t = 0; t < 3; ++t) { st[t][0] = mfma32(kf[ks][t], qf[ks][0], st[t][0]); st[t][1] = mfma32(kf[ks][t], qf[ks][1], st[t][1]); }
    bf16x8 px[2], pm[2];
#pragma unroll
    for (int qt = 0; qt < 2; ++qt) {
        const int qi = qt * 16 + c16;
        float x[3][4]; float mx = -INFINITY;
#pragma unroll
        for (int t = 0; t < 3; ++t)
#pragma unroll
            for (int r = 0; r < 4; ++r) {
                const float sv = st[t][qt][r];
                float v = rbf(rbf(sv) * scale);
                const int key = (t - 1) * 16 + 4 * g + r;
                const bool valid = (t == 0) ? (g < 2) : (key <= qi);
                if (!valid) v = -INFINITY;
                x[t][r] = v; mx = fmaxf(mx, v);
            }
        mx = xmax16_32(mx);
        float sum = 0.f;
#pragma unroll
        for (int t = 0; t < 3; ++t)
#pragma unroll
            for (int r = 0; r < 4; ++r) { x[t][r] = __expf(x[t][r] - mx); sum += x[t][r]; }
        sum = xsum16_32(sum);
        const float inv = 1.0f / sum;
#pragma unroll
        for (int r = 0; r < 4; ++r) {
            pm[qt][r] = (short)f2bf(x[0][r] * inv); pm[qt][4 + r] = 0;
            px[qt][r] = (short)f2bf(x[1][r] * inv);
            px[qt][4 + r] = (short)f2bf(x[2][r] * inv);
        }
    }
#pragma unroll
    for (int et = 0; et < 8; ++et) {
        const bf16x8 vx = vxf[et], vm = vmf[et];
#pragma unroll
        for (int qt = 0; qt < 2; ++qt) {
            f32x4 o = mfma32(vx, px[qt], (f32x4){0.f, 0.f, 0.f, 0.f});
            o = mfma32(vm, pm[qt], o);
            const float o0 = o[0], o1 = o[1], o2 = o[2], o3 = o[3];
            v2u pk;
            pk.x = (unsigned)f2bf(o0) | ((unsigned)f2bf(o1) << 16);
            pk.y = (unsigned)f2bf(o2) | ((unsigned)f2bf(o3) << 16);
            st8(O + (size_t)(s * SEG + qt * 16 + c16) * HD + h * DH + et * 16 + 4 * g, pk);
        }
    }
}

__device__ __forceinline__ void oproj_task(const Args& a, int s, int ns, unsigned char* lds) {
    const int tid = opaque_tid(), w = tid >> 6, lane = tid & 63, c16 = lane & 15, g = lane >> 4;
    const u16* O = (const u16*)(a.ws + O_OFF) + (size_t)(s * SEG + c16) * HD + w * 1024 + 8 * g;
    const u16* Wt = (const u16*)(a.ws + WOT_OFF) + (size_t)(ns * 64 + c16) * HD + w * 1024 + 8 * g;
    f32x4 acc[4][2];
#pragma unroll
    for (int nt = 0; nt < 4; ++nt) { acc[nt][0] = (f32x4){0.f, 0.f, 0.f, 0.f}; acc[nt][1] = (f32x4){0.f, 0.f, 0.f, 0.f}; }
#pragma unroll 1
    for (int kb = 0; kb < 4; ++kb) {
        bf16x8 of[8][2], wf[8][4];
#pragma unroll
        for (int k8 = 0; k8 < 8; ++k8) {
            const int ko = (kb * 8 + k8) * 32;
            of[k8][0] = *(const bf16x8*)(O + ko); of[k8][1] = *(const bf16x8*)(O + (size_t)16 * HD + ko);
#pragma unroll
            for (int nt = 0; nt < 4; ++nt) wf[k8][nt] = *(const bf16x8*)(Wt + (size_t)nt * 16 * HD + ko);
        }
        __builtin_amdgcn_sched_barrier(0);
#pragma unroll
        for (int k8 = 0; k8 < 8; ++k8)
#pragma unroll
            for (int nt = 0; nt < 4; ++nt) {
                acc[nt][0] = mfma32(wf[k8][nt], of[k8][0], acc[nt][0]);
                acc[nt][1] = mfma32(wf[k8][nt], of[k8][1], acc[nt][1]);
            }
    }
    f32x4* R = (f32x4*)lds;
#pragma unroll
    for (int nt = 0; nt < 4; ++nt) { R[(w * 8 + nt * 2) * 64 + lane] = acc[nt][0]; R[(w * 8 + nt * 2 + 1) * 64 + lane] = acc[nt][1]; }
    __syncthreads();
#pragma unroll
    for (int i = 0; i < 2; ++i) {
        const int tile = w * 2 + i, nt = tile >> 1, mt = tile & 1;
        const f32x4 v = (R[tile * 64 + lane] + R[(8 + tile) * 64 + lane]) + (R[(16 + tile) * 64 + lane] + R[(24 + tile) * 64 + lane]);
        const float v0 = v[0], v1 = v[1], v2 = v[2], v3 = v[3];
        v2u pk;
        pk.x = (unsigned)f2bf(v0) | ((unsigned)f2bf(v1) << 16);
        pk.y = (unsigned)f2bf(v2) | ((unsigned)f2bf(v3) << 16);
        *(v2u*)(a.out + (size_t)(s * SEG + mt * 16 + c16) * (2 * DM) + ns * 64 + nt * 16 + 4 * g) = pk;
    }
    __syncthreads();
}

__device__ __forceinline__ void oproj_fine_task(const Args& a, int s, int n16, unsigned char* lds, gu32* tmo) {
    const int tid = opaque_tid(), w = tid >> 6, lane = tid & 63, c16 = lane & 15, g = lane >> 4;
    const u16* O = (const u16*)(a.ws + O_OFF) + (size_t)(s * SEG + c16) * HD + w * 1024 + 8 * g;
    const u16* Wt = (const u16*)(a.ws + WOT_OFF) + (size_t)(n16 * 16 + c16) * HD + w * 1024 + 8 * g;
    wait_flags(flagp(a.ws, F_TR), 740u, tid == 0, tmo);
    bf16x8 wf[32];
#pragma unroll
    for (int ks = 0; ks < 32; ++ks) wf[ks] = *(const bf16x8*)(Wt + ks * 32);
    wait_flags(flagp(a.ws, F_ATT + s), 8u, tid == 0, tmo);
    f32x4 acc[2] = {(f32x4){0.f, 0.f, 0.f, 0.f}, (f32x4){0.f, 0.f, 0.f, 0.f}};
#pragma unroll
    for (int kb = 0; kb < 2; ++kb) {
        bf16x8 of[16][2];
#pragma unroll
        for (int k = 0; k < 16; ++k) { of[k][0] = *(const bf16x8*)(O + (kb * 16 + k) * 32); of[k][1] = *(const bf16x8*)(O + (size_t)16 * HD + (kb * 16 + k) * 32); }
        __builtin_amdgcn_sched_barrier(0);
#pragma unroll
        for (int k = 0; k < 16; ++k) { acc[0] = mfma32(wf[kb * 16 + k], of[k][0], acc[0]); acc[1] = mfma32(wf[kb * 16 + k], of[k][1], acc[1]); }
    }
    f32x4* R = (f32x4*)lds;
    R[(w * 2) * 64 + lane] = acc[0]; R[(w * 2 + 1) * 64 + lane] = acc[1];
    __syncthreads();
    if (w < 2) {
        const f32x4 v = (R[w * 64 + lane] + R[(2 + w) * 64 + lane]) + (R[(4 + w) * 64 + lane] + R[(6 + w) * 64 + lane]);
        const float v0 = v[0], v1 = v[1], v2 = v[2], v3 = v[3];
        v2u pk;
        pk.x = (unsigned)f2bf(v0) | ((unsigned)f2bf(v1) << 16);
        pk.y = (unsigned)f2bf(v2) | ((unsigned)f2bf(v3) << 16);
        *(v2u*)(a.out + (size_t)(s * SEG + w * 16 + c16) * (2 * DM) + n16 * 16 + 4 * g) = pk;
    }
    __syncthreads();
}

__device__ __forceinline__ void bfinal_task(const Args& a, int rb32, unsigned char* lds) {
    const int tid = opaque_tid(), w = tid >> 6, lane = tid & 63, c16 = lane & 15, g = lane >> 4;
    const u16* Bq = (const u16*)(a.ws + BQKV_OFF);
    constexpr int OST = 1296;
    const float scale = 0.22360679774997896f;
    for (int it = 0; it < 4; ++it) {
        const int idx = it * 256 + tid, h = idx & 31, row = idx >> 5, pos = rb32 * 32 + row;
        const u16* qp = Bq + (size_t)pos * BD3 + h * BDH;
        float q[BDH];
#pragma unroll
        for (int d = 0; d < BDH; d += 2) { const unsigned u = *(const unsigned*)(qp + d); q[d] = bf2f((u16)u); q[d + 1] = bf2f((u16)(u >> 16)); }
        float lg[8]; float mx = -INFINITY;
#pragma unroll
        for (int wi = 0; wi < 8; ++wi) {
            const int kp = pos - 2 * wi;
            float v = -INFINITY;
            if (kp >= 0) {
                const u16* kpn = Bq + (size_t)kp * BD3 + DM + h * BDH;
                float d0 = 0.f;
#pragma unroll
                for (int d = 0; d < BDH; d += 2) { const unsigned u = *(const unsigned*)(kpn + d); d0 += q[d] * bf2f((u16)u); d0 += q[d + 1] * bf2f((u16)(u >> 16)); }
                v = rbf(rbf(d0) * scale);
            }
            lg[wi] = v; mx = fmaxf(mx, v);
        }
        float sum = 0.f;
#pragma unroll
        for (int wi = 0; wi < 8; ++wi) { lg[wi] = __expf(lg[wi] - mx); sum += lg[wi]; }
        const float inv = 1.0f / sum;
        float o[BDH];
#pragma unroll
        for (int d = 0; d < BDH; ++d) o[d] = 0.f;
#pragma unroll
        for (int wi = 0; wi < 8; ++wi) {
            const int kp = pos - 2 * wi;
            if (kp >= 0) {
                const float p = rbf(lg[wi] * inv);
                const u16* vpn = Bq + (size_t)kp * BD3 + 2 * DM + h * BDH;
#pragma unroll
                for (int d = 0; d < BDH; d += 2) { const unsigned u = *(const unsigned*)(vpn + d); o[d] += p * bf2f((u16)u); o[d + 1] += p * bf2f((u16)(u >> 16)); }
            }
        }
#pragma unroll
        for (int d = 0; d < BDH; d += 2) *(unsigned*)(lds + row * OST + (h * BDH + d) * 2) = (unsigned)f2bf(o[d]) | ((unsigned)f2bf(o[d + 1]) << 16);
    }
    __syncthreads();
    const u16* Wt = (const u16*)(a.ws + BWOT_OFF) + (size_t)(w * 160 + c16) * DM + 8 * g;
#pragma unroll 1
    for (int nh = 0; nh < 2; ++nh) {
        f32x4 acc[5][2];
#pragma unroll
        for (int nt = 0; nt < 5; ++nt) { acc[nt][0] = (f32x4){0.f, 0.f, 0.f, 0.f}; acc[nt][1] = (f32x4){0.f, 0.f, 0.f, 0.f}; }
#pragma unroll 4
        for (int ks = 0; ks < 20; ++ks) {
            const bf16x8 o0 = *(const bf16x8*)(lds + c16 * OST + ks * 64 + g * 16), o1 = *(const bf16x8*)(lds + (16 + c16) * OST + ks * 64 + g * 16);
#pragma unroll
            for (int nt = 0; nt < 5; ++nt) {
                const bf16x8 wf = *(const bf16x8*)(Wt + (size_t)(nh * 5 + nt) * 16 * DM + ks * 32);
                acc[nt][0] = mfma32(wf, o0, acc[nt][0]);
                acc[nt][1] = mfma32(wf, o1, acc[nt][1]);
            }
        }
#pragma unroll
        for (int nt = 0; nt < 5; ++nt)
#pragma unroll
            for (int mt = 0; mt < 2; ++mt) {
                const float v0 = acc[nt][mt][0], v1 = acc[nt][mt][1], v2 = acc[nt][mt][2], v3 = acc[nt][mt][3];
                v2u pk;
                pk.x = (unsigned)f2bf(v0) | ((unsigned)f2bf(v1) << 16);
                pk.y = (unsigned)f2bf(v2) | ((unsigned)f2bf(v3) << 16);
                *(v2u*)(a.out + (size_t)(rb32 * 32 + mt * 16 + c16) * (2 * DM) + DM + w * 160 + (nh * 5 + nt) * 16 + 4 * g) = pk;
            }
    }
    __syncthreads();
}

constexpr int NWORK = 192, NCOARSE = 56;
constexpr int T_KV = 0, T_TR = T_KV + 1024, T_Q = T_TR + 740, T_BQ = T_Q + 512, T_BF = T_BQ + 240, T_SEG = T_BF + 64, T_LAST = T_SEG + NCOARSE * 18, T_END = T_LAST + (NSEG - NCOARSE) * 48;

__device__ __forceinline__ void worker_role(const Args& a, unsigned char* lds) {
    const int wid = blockIdx.x - 64, tid = threadIdx.x, w = tid >> 6;
    gu32* tmo = (gu32*)(a.ws + TMO_OFF);
    u16* Q = (u16*)(a.ws + Q_OFF); u16* K = (u16*)(a.ws + K_OFF); u16* Vt = (u16*)(a.ws + VT_OFF); u16* Bq = (u16*)(a.ws + BQKV_OFF);
    for (int T = wid; T < T_END; T += NWORK) {
        if (T < T_TR) {
            const int rb = T >> 6, r = T & 63, hc = r & 31;
            if (r < 32) gemm_tile<0>(a.Ax, DM, a.Wk, HD, K, HD, DM, rb * 128, hc * 128, lds);
            else gemm_tile<1>(a.Ax, DM, a.Wv, HD, Vt, HD, DM, rb * 128, hc * 128, lds);
            signal_flag(flagp(a.ws, F_KV + rb * 32 + hc));
        } else if (T < T_Q) {
            const int t = T - T_TR;
            if (t < 640) transpose_tile(a.Wo, DM, (u16*)(a.ws + WOT_OFF), HD, (t / 10) * 64, (t % 10) * 64, lds, a.ws);
            else transpose_tile(a.Bwo, DM, (u16*)(a.ws + BWOT_OFF), DM, ((t - 640) / 10) * 64, ((t - 640) % 10) * 64, lds, a.ws);
            signal_flag(flagp(a.ws, F_TR));
        } else if (T < T_BQ) {
            const int t = T - T_Q, rb = t >> 5, hc = t & 31;
            gemm_tile<0>(a.Ax, DM, a.Wq, HD, Q, HD, DM, rb * 128, hc * 128, lds);
            signal_flag(flagp(a.ws, F_Q + rb * 32 + hc));
        } else if (T < T_BF) {
            const int t = T - T_BQ, rb = t / 15, nt = t - rb * 15;
            gemm_tile<0>(a.Bx, DM, a.Bw, BD3, Bq, BD3, DM, rb * 128, nt * 128, lds);
            signal_flag(flagp(a.ws, F_BQ + rb));
        } else if (T < T_SEG) {
            const int rb32 = T - T_BF, rb = rb32 >> 2;
            const int fi = tid == 0 ? F_BQ + rb : (tid == 1 ? F_BQ + (rb > 0 ? rb - 1 : 0) : F_TR);
            wait_flags(flagp(a.ws, fi), tid < 2 ? 15u : 740u, tid < 3, tmo);
            bfinal_task(a, rb32, lds);
        } else {
            const int t = T - T_SEG, s = T < T_LAST ? t / 18 : NCOARSE + (T - T_LAST) / 48, r = T < T_LAST ? t - s * 18 : (T - T_LAST) % 48, rb = s >> 2;
            if (T >= T_LAST && r >= 8) {
                oproj_fine_task(a, s, r - 8, lds, tmo);
            } else if (r < 8) {
                const int hh = r * 4 + (tid & 3), kind = (tid >> 2) & 1;
                wait_flags(flagp(a.ws, kind == 0 ? F_KV + rb * 32 + hh : F_Q + rb * 32 + hh), kind == 1 ? 1u : 2u, tid < 8, tmo);
                attn_x(a, s, r * 4 + w);
                signal_flag(flagp(a.ws, F_ATT + s));
            } else {
                wait_flags(flagp(a.ws, tid == 0 ? F_ATT + s : F_TR), tid == 0 ? 8u : 740u, tid < 2, tmo);
                oproj_task(a, s, r - 8, lds);
            }
        }
    }
}

__global__ __launch_bounds__(256, 1) void fused_kernel(Args a) {
    extern __shared__ __attribute__((aligned(16))) unsigned char lds[];
    if (blockIdx.x < 64) chain_role(a, lds);
    else worker_role(a, lds);
}

extern "C" void kernel_launch(void* const* d_in, const int* in_sizes, int n_in,
                              void* d_out, int out_size, void* d_ws, size_t ws_size,
                              hipStream_t stream) {
    const u16* Ax = (const u16*)d_in[0];
    const u16* mem0 = (const u16*)d_in[1];
    const u16* Wq = (const u16*)d_in[2];
    const u16* Wk = (const u16*)d_in[3];
    const u16* Wv = (const u16*)d_in[4];
    const u16* Wo = (const u16*)d_in[5];
    const u16* Bx = (const u16*)d_in[7];
    const u16* Bw = (const u16*)d_in[8];
    const u16* Bwo = (const u16*)d_in[9];
    unsigned char* ws = (unsigned char*)d_ws;
    u16* out = (u16*)d_out;

    (void)hipMemsetAsync(ws, 0, XCHG_BYTES, stream);
    Args a{Ax, mem0, Wq, Wk, Wv, Wo, Bx, Bw, Bwo, out, ws};
    (void)hipFuncSetAttribute((const void*)fused_kernel, hipFuncAttributeMaxDynamicSharedMemorySize, CHAIN_LDS);
    fused_kernel<<<256, 256, CHAIN_LDS, stream>>>(a);
}
```

```cpp
#include <hip/hip_runtime.h>
#include <hip/hip_bf16.h>
#include <stdint.h>

#define GAS __attribute__((address_space(1)))
#define LAS __attribute__((address_space(3)))
typedef unsigned short u16;
typedef short bf16x8 __attribute__((ext_vector_type(8)));
typedef short bf16x4 __attribute__((ext_vector_type(4)));
typedef float f32x4 __attribute__((ext_vector_type(4)));
typedef unsigned v4u __attribute__((ext_vector_type(4)));
typedef unsigned v2u __attribute__((ext_vector_type(2)));
typedef GAS unsigned long long gu64;
typedef GAS unsigned gu32;
typedef LAS bf16x4 lds_bf16x4;
#define RLX_AGENT __ATOMIC_RELAXED, __HIP_MEMORY_SCOPE_AGENT

constexpr int SEQ = 2048, DM = 640, NH = 32, DH = 128, MEMN = 8, SEG = 32, NSEG = 64, HD = 4096;
constexpr int BD3 = 1920, BDH = 20;

constexpr size_t XA_OFF = 0;
constexpr size_t XA_BYTES = (size_t)2 * 64 * 512 * 8;
constexpr size_t XB_OFF = XA_OFF + XA_BYTES;
constexpr size_t XB_BYTES = (size_t)2 * 64 * 2560 * 8;
constexpr size_t XC_OFF = XB_OFF + XB_BYTES;
constexpr int XC_REP = 1;
constexpr size_t XC_BYTES = (size_t)2 * XC_REP * 2560 * 8;
constexpr size_t TMO_OFF = XC_OFF + XC_BYTES;
constexpr size_t FL_OFF = TMO_OFF + 256;
constexpr int F_KV = 0, F_Q = F_KV + 512, F_KM = F_Q + 512, F_ATT = F_KM + 2048, F_BQ = F_ATT + 64, F_TR = F_BQ + 16, F_END = F_TR + 1;
constexpr size_t XCHG_BYTES = (FL_OFF + (size_t)F_END * 64 + 255) / 256 * 256;
constexpr size_t Q_OFF = (XCHG_BYTES + 4095) / 4096 * 4096;
constexpr size_t K_OFF = Q_OFF + (size_t)SEQ * HD * 2;
constexpr size_t VT_OFF = K_OFF + (size_t)SEQ * HD * 2;
constexpr size_t KM_OFF = VT_OFF + (size_t)SEQ * HD * 2;
constexpr size_t VM_OFF = KM_OFF + (size_t)NSEG * MEMN * HD * 2;
constexpr size_t O_OFF = VM_OFF + (size_t)NSEG * MEMN * HD * 2;
constexpr size_t BQKV_OFF = O_OFF + (size_t)SEQ * HD * 2;
constexpr size_t WOT_OFF = BQKV_OFF + (size_t)SEQ * BD3 * 2;
constexpr size_t BWOT_OFF = WOT_OFF + (size_t)DM * HD * 2;
constexpr size_t WS_END = BWOT_OFF + (size_t)DM * DM * 2;

__device__ __forceinline__ int opaque_tid() { int t = threadIdx.x; asm volatile("" : "+v"(t)); return t; }
__device__ __forceinline__ u16 f2bf(float f) { return __builtin_bit_cast(u16, (__bf16)f); }
__device__ __forceinline__ float bf2f(u16 b) { return __builtin_bit_cast(float, (unsigned)b << 16); }
__device__ __forceinline__ float rbf(float f) { return bf2f(f2bf(f)); }
typedef float f32x2 __attribute__((ext_vector_type(2)));
typedef __bf16 bf16v2 __attribute__((ext_vector_type(2)));
__device__ __forceinline__ unsigned pk2bf(float a, float b) { const f32x2 v = {a, b}; return __builtin_bit_cast(unsigned, __builtin_convertvector(v, bf16v2)); }
__device__ __forceinline__ f32x4 mfma32(bf16x8 a, bf16x8 b, f32x4 c) { return __builtin_amdgcn_mfma_f32_16x16x32_bf16(a, b, c, 0, 0, 0); }

__device__ __forceinline__ void st8(u16* p, v2u v) { __hip_atomic_store((gu64*)p, (unsigned long long)v.x | ((unsigned long long)v.y << 32), RLX_AGENT); }
__device__ __forceinline__ unsigned off_b(unsigned row, unsigned ch) { return 256u * row + 16u * (ch ^ (((row & 3) << 2) | ((row >> 2) & 3))); }

template <int MODE>
__device__ __forceinline__ void gemm_tile(const u16* __restrict__ A, int lda, const u16* __restrict__ W, int ldw,
                                          u16* __restrict__ C, int ldc, int K, int m0, int n0, unsigned char* lds) {
    const int tid = opaque_tid(), w = tid >> 6, lane = tid & 63, c16 = lane & 15, g = lane >> 4;
    const int wm = w >> 1, wn = w & 1;
    unsigned char* LA = lds;
    unsigned char* LW = lds + 32768;
    f32x4 acc[4][4];
#pragma unroll
    for (int i = 0; i < 4; ++i)
#pragma unroll
        for (int jn = 0; jn < 4; ++jn) acc[i][jn] = (f32x4){0.f, 0.f, 0.f, 0.f};
    constexpr int NK = 10, RD = 4;
    v4u ra[RD][4], rw[RD][4];
    const u16* Ap = A + (size_t)(m0 + (tid >> 3)) * lda + (tid & 7) * 8;
    const u16* Wp = W + (size_t)(tid >> 4) * ldw + n0 + (tid & 15) * 8;
    const unsigned q4 = (lane & 15) >> 2, p4 = lane & 3;
#pragma unroll
    for (int kt = 0; kt < RD; ++kt)
#pragma unroll
        for (int i = 0; i < 4; ++i) {
            ra[kt][i] = *(const v4u*)(Ap + (size_t)i * 32 * lda + kt * 64);
            rw[kt][i] = *(const v4u*)(Wp + (size_t)(kt * 64 + i * 16) * ldw);
        }
#pragma unroll
    for (int kt = 0; kt < NK; ++kt) {
        const int buf = kt & 1, sl = kt % RD;
#pragma unroll
        for (int i = 0; i < 4; ++i) {
            const int c = tid + 256 * i;
            const int row = c >> 3, ch = c & 7;
            *(v4u*)(LA + buf * 16384 + row * 128 + 16 * (ch ^ (row & 7))) = ra[sl][i];
            const int r2 = c >> 4, ch2 = c & 15;
            *(v4u*)(LW + buf * 16384 + (r2 >> 5) * 8192 + off_b(r2 & 31, ch2)) = rw[sl][i];
        }
        __syncthreads();
        if (kt + RD < NK) {
#pragma unroll
            for (int i = 0; i < 4; ++i) {
                ra[sl][i] = *(const v4u*)(Ap + (size_t)i * 32 * lda + (kt + RD) * 64);
                rw[sl][i] = *(const v4u*)(Wp + (size_t)((kt + RD) * 64 + i * 16) * ldw);
            }
        }
#pragma unroll
        for (int ks = 0; ks < 2; ++ks) {
            bf16x8 af[4], wf[4];
#pragma unroll
            for (int mt = 0; mt < 4; ++mt) {
                const int row = wm * 64 + mt * 16 + c16, ch = ks * 4 + g;
                af[mt] = *(const bf16x8*)(LA + buf * 16384 + row * 128 + 16 * (ch ^ (row & 7)));
            }
#pragma unroll
            for (int nt = 0; nt < 4; ++nt) {
                const unsigned cb = wn * 4 + nt;
                unsigned char* base = LW + buf * 16384 + ks * 8192;
                const bf16x4 lo = __builtin_amdgcn_ds_read_tr16_b64_v4i16((lds_bf16x4*)(base + off_b(8 * g + q4, 2 * cb + (p4 >> 1)) + 8 * (p4 & 1)));
                const bf16x4 hi = __builtin_amdgcn_ds_read_tr16_b64_v4i16((lds_bf16x4*)(base + off_b(8 * g + 4 + q4, 2 * cb + (p4 >> 1)) + 8 * (p4 & 1)));
                wf[nt] = __builtin_shufflevector(lo, hi, 0, 1, 2, 3, 4, 5, 6, 7);
            }
#pragma unroll
            for (int mt = 0; mt < 4; ++mt)
#pragma unroll
                for (int nt = 0; nt < 4; ++nt)
                    acc[mt][nt] = (MODE == 0) ? mfma32(wf[nt], af[mt], acc[mt][nt]) : mfma32(af[mt], wf[nt], acc[mt][nt]);
        }
    }
    __syncthreads();
#pragma unroll
    for (int mt = 0; mt < 4; ++mt)
#pragma unroll
        for (int nt = 0; nt < 4; ++nt) {
            v2u pk;
            pk.x = (unsigned)f2bf(acc[mt][nt][0]) | ((unsigned)f2bf(acc[mt][nt][1]) << 16);
            pk.y = (unsigned)f2bf(acc[mt][nt][2]) | ((unsigned)f2bf(acc[mt][nt][3]) << 16);
            if (MODE == 0) {
                const int m = m0 + wm * 64 + mt * 16 + c16, n = n0 + wn * 64 + nt * 16 + 4 * g;
                st8(C + (size_t)m * ldc + n, pk);
            } else {
                const int m = m0 + wm * 64 + mt * 16 + 4 * g, n = n0 + wn * 64 + nt * 16 + c16;
                st8(C + ((size_t)(m >> 5) * HD + n) * 32 + 8 * g + 4 * ((m >> 4) & 1), pk);
            }
        }
}

struct Args { const u16* Ax; const u16* mem0; const u16* Wq; const u16* Wk; const u16* Wv; const u16* Wo;
              const u16* Bx; const u16* Bw; const u16* Bwo; u16* out; unsigned char* ws; };

constexpr unsigned kSpinCap = 1u << 17;
#define SLEEP_A 0
#define SLEEP_B 16
#define SLEEP_C 16

typedef __amdgpu_buffer_rsrc_t rsrc_t;
__device__ __forceinline__ void st16(rsrc_t rs, int off, v4u v) { __builtin_amdgcn_raw_buffer_store_b128(v, rs, off, 0, 16); }
template <int NPT, bool TAG16>
__device__ __forceinline__ void gather(rsrc_t rs, const int (&off)[NPT], unsigned tag, v4u (&v)[NPT], gu32* tmo) {
    for (unsigned spins = 0;; ++spins) {
        asm volatile("" ::: "memory");
#pragma unroll
        for (int k = 0; k < NPT; ++k) v[k] = __builtin_amdgcn_raw_buffer_load_b128(rs, off[k], 0, 16);
        bool ok = true;
#pragma unroll
        for (int k = 0; k < NPT; ++k) {
            if (TAG16) ok &= ((v[k].y >> 16) == tag) & ((v[k].w >> 16) == tag);
            else ok &= (v[k].y == tag) & (v[k].w == tag);
        }
        if (__all(ok)) break;
        if ((spins & 63u) == 63u) {
            if (spins > kSpinCap) { __hip_atomic_store(tmo, 1u, RLX_AGENT); break; }
            if (__hip_atomic_load(tmo, RLX_AGENT) != 0u) break;
        }
    }
}
__device__ __forceinline__ float xmax16_32(float v) {
    auto r = __builtin_amdgcn_permlane16_swap(__float_as_uint(v), __float_as_uint(v), false, false);
    v = fmaxf(__uint_as_float(r[0]), __uint_as_float(r[1]));
    auto r2 = __builtin_amdgcn_permlane32_swap(__float_as_uint(v), __float_as_uint(v), false, false);
    return fmaxf(__uint_as_float(r2[0]), __uint_as_float(r2[1]));
}
__device__ __forceinline__ float xsum16_32(float v) {
    auto r = __builtin_amdgcn_permlane16_swap(__float_as_uint(v), __float_as_uint(v), false, false);
    v = __uint_as_float(r[0]) + __uint_as_float(r[1]);
    auto r2 = __builtin_amdgcn_permlane32_swap(__float_as_uint(v), __float_as_uint(v), false, false);
    return __uint_as_float(r2[0]) + __uint_as_float(r2[1]);
}

constexpr int MEM_STRIDE = 1312, QK_STRIDE = 288;
constexpr int L_WO = 0;
constexpr int L_MEM = L_WO + 81920, L_Q = L_MEM + 8 * MEM_STRIDE, L_K = L_Q + 8 * QK_STRIDE, L_VT = L_K + 8 * QK_STRIDE,
              L_RED = L_VT + 128 * 32, L_KV = L_RED + 6 * 80 * 4, L_END = L_KV + 16 * 1024;
constexpr int L_STAGE = L_MEM;
constexpr int CHAIN_LDS = (L_STAGE + 51200 > L_END) ? (L_STAGE + 51200) : L_END;

__device__ __forceinline__ gu32* flagp(unsigned char* ws, int idx) { return (gu32*)(ws + FL_OFF + (size_t)idx * 64); }

__device__ __forceinline__ void chain_role(const Args& a, unsigned char* lds) {
    const int b_ = blockIdx.x, cu = ((b_ & 7) + 8 * (b_ >> 4)) * 2 + ((b_ >> 3) & 1), h = cu >> 1, j = cu & 1;
    const int tid = threadIdx.x, w = tid >> 6, lane = tid & 63, c16 = lane & 15, g = lane >> 4;
    gu32* tmo = (gu32*)(a.ws + TMO_OFF);
    const rsrc_t xr = __builtin_amdgcn_make_buffer_rsrc((void*)a.ws, 0, (int)TMO_OFF, 0x00020000);
    const rsrc_t kr_ = __builtin_amdgcn_make_buffer_rsrc((void*)(a.ws + K_OFF), 0, SEQ * HD * 2, 0x00020000);
    const rsrc_t vr_ = __builtin_amdgcn_make_buffer_rsrc((void*)(a.ws + VT_OFF), 0, SEQ * HD * 2, 0x00020000);
    u16* Km = (u16*)(a.ws + KM_OFF);
    u16* Vm = (u16*)(a.ws + VM_OFF);

    bf16x8 wqkv[3][20];
    {
        unsigned char* stg = lds + L_STAGE;
        const u16* Wqk = j ? a.Wk : a.Wq;
        const unsigned q4 = (lane & 15) >> 2, p4 = lane & 3;
        v4u tmp[2][12];
        auto issue = [&](int rd, v4u (&t)[12]) {
            if (rd < 5) {
#pragma unroll
                for (int i = 0; i < 12; ++i) {
                    const int c = tid + 256 * i;
                    const int row = c / 24, ch = c - row * 24;
                    const size_t kr = (size_t)(rd * 128 + row) * HD + h * DH;
                    t[i] = (ch < 16) ? *(const v4u*)(Wqk + kr + ch * 8) : *(const v4u*)(a.Wv + kr + j * 64 + (ch - 16) * 8);
                }
            } else {
#pragma unroll
                for (int i = 0; i < 10; ++i) {
                    const int c = tid + 256 * i;
                    const int row = c / 40, ch = c - row * 40;
                    t[i] = *(const v4u*)(a.Wo + (size_t)(h * DH + (rd - 5) * 64 + row) * DM + j * 320 + ch * 8);
                }
            }
        };
        issue(0, tmp[0]);
#pragma unroll
        for (int rd = 0; rd < 7; ++rd) {
            if (rd + 1 < 7) issue(rd + 1, tmp[(rd + 1) & 1]);
            if (rd < 5) {
#pragma unroll
                for (int i = 0; i < 12; ++i) {
                    const int c = tid + 256 * i;
                    const int row = c / 24, ch = c - row * 24;
                    *(v4u*)(stg + row * 400 + ch * 16) = tmp[rd & 1][i];
                }
            } else {
#pragma unroll
                for (int i = 0; i < 10; ++i) {
                    const int c = tid + 256 * i;
                    const int row = c / 40, ch = c - row * 40;
                    *(v4u*)(stg + row * 656 + ch * 16) = tmp[rd & 1][i];
                }
            }
            asm volatile("s_waitcnt lgkmcnt(0)" ::: "memory");
            __builtin_amdgcn_s_barrier();
            if (rd < 5) {
#pragma unroll
                for (int k4 = 0; k4 < 4; ++k4)
#pragma unroll
                    for (int t = 0; t < 3; ++t) {
                        const int col = (t < 2) ? ((2 * w + t) * 16) : (128 + w * 16);
                        unsigned char* p0 = stg + (k4 * 32 + 8 * g + q4) * 400 + (col + 4 * p4) * 2;
                        const bf16x4 lo = __builtin_amdgcn_ds_read_tr16_b64_v4i16((lds_bf16x4*)p0);
                        const bf16x4 hi = __builtin_amdgcn_ds_read_tr16_b64_v4i16((lds_bf16x4*)(p0 + 4 * 400));
                        wqkv[t][(rd < 5 ? rd : 0) * 4 + k4] = __builtin_shufflevector(lo, hi, 0, 1, 2, 3, 4, 5, 6, 7);
                    }
            } else {
#pragma unroll
                for (int k2 = 0; k2 < 2; ++k2)
#pragma unroll
                    for (int nt = 0; nt < 5; ++nt) {
                        unsigned char* p0 = stg + (k2 * 32 + 4 * g + q4) * 656 + (w * 80 + nt * 16 + 4 * p4) * 2;
                        const bf16x4 lo = __builtin_amdgcn_ds_read_tr16_b64_v4i16((lds_bf16x4*)p0);
                        const bf16x4 hi = __builtin_amdgcn_ds_read_tr16_b64_v4i16((lds_bf16x4*)(p0 + 16 * 656));
                        *(bf16x8*)(lds + L_WO + ((w * 5 + nt) * 4 + (rd - 5) * 2 + k2) * 1024 + lane * 16) = __builtin_shufflevector(lo, hi, 0, 1, 2, 3, 4, 5, 6, 7);
                    }
            }
            asm volatile("s_waitcnt lgkmcnt(0)" ::: "memory");
            __builtin_amdgcn_s_barrier();
        }
        if (tid < 128) *(v4u*)(lds + L_VT + tid * 32 + 16) = (v4u){0u, 0u, 0u, 0u};
    }
    const float scale = 0.08838834764831845f;
    const int tt = tid < 240 ? tid : 239, hg = tt / 40, pr = tt - hg * 40;

    for (int s = 0; s < NSEG; ++s) {
        const unsigned tag = (unsigned)(s + 1);
        const int par = s & 1;
        if ((s & 3) == 0) {
            gu32* fl = flagp(a.ws, F_KV + (s >> 2) * 32 + h);
            for (unsigned spins = 0;; ++spins) {
                if (__builtin_amdgcn_readfirstlane(__hip_atomic_load(fl, RLX_AGENT)) >= 2u) break;
                __builtin_amdgcn_s_sleep(4);
                if ((spins & 63u) == 63u) {
                    if (spins > kSpinCap) { __hip_atomic_store(tmo, 1u, RLX_AGENT); break; }
                    if (__hip_atomic_load(tmo, RLX_AGENT) != 0u) break;
                }
            }
            __builtin_amdgcn_fence(__ATOMIC_ACQUIRE, "wavefront");
        }
        v4u kvq[4];
#pragma unroll
        for (int i = 0; i < 4; ++i) {
            if (w < 2) kvq[i] = __builtin_amdgcn_raw_buffer_load_b128(kr_, ((s * SEG + w * 16 + c16) * HD + h * DH + 32 * i + 8 * g) * 2, 0, 16);
            else kvq[i] = __builtin_amdgcn_raw_buffer_load_b128(vr_, ((s * HD + h * DH + ((w - 2) * 4 + i) * 16 + c16) * 32 + 8 * g) * 2, 0, 16);
        }
        if (s == 0) {
            for (int i = tid; i < 8 * 80; i += 256) {
                const int tk = i / 80, ch = i % 80;
                *(v4u*)(lds + L_MEM + tk * MEM_STRIDE + ch * 16) = *(const v4u*)(a.mem0 + tk * DM + ch * 8);
            }
        } else {
            v4u v[5];
            __builtin_amdgcn_s_sleep(SLEEP_C);
            const int o0 = (int)XC_OFF + (((s - 1) & 1) * XC_REP + (cu & (XC_REP - 1))) * 20480 + tid * 16;
            const int offs[5] = {o0, o0 + 4096, o0 + 8192, o0 + 12288, o0 + 16384};
            gather<5, false>(xr, offs, (unsigned)s, v, tmo);
            {
                const int l2 = (2 * tid) & 63;
                const int tok = l2 & 7, col = ((tid >> 5) * 16 + (l2 >> 4) * 4 + ((l2 >> 3) & 1) * 2) * 2;
#pragma unroll
                for (int k = 0; k < 5; ++k) {
                    *(unsigned*)(lds + L_MEM + tok * MEM_STRIDE + col + k * 256) = v[k].x;
                    *(unsigned*)(lds + L_MEM + (tok + 1) * MEM_STRIDE + col + k * 256) = v[k].z;
                }
            }
        }
#pragma unroll
        for (int i = 0; i < 4; ++i) *(v4u*)(lds + L_KV + (w * 4 + i) * 1024 + lane * 16) = kvq[i];
        __syncthreads();
        f32x4 acc[3];
        {
            bf16x8 mb[20];
#pragma unroll
            for (int ks = 0; ks < 20; ++ks) mb[ks] = *(const bf16x8*)(lds + L_MEM + (c16 & 7) * MEM_STRIDE + ks * 64 + g * 16);
            __builtin_amdgcn_sched_barrier(0);
#pragma unroll
            for (int t = 0; t < 3; ++t) acc[t] = (f32x4){0.f, 0.f, 0.f, 0.f};
#pragma unroll
            for (int ks = 0; ks < 20; ++ks)
#pragma unroll
                for (int t = 0; t < 3; ++t) acc[t] = mfma32(wqkv[t][ks], mb[ks], acc[t]);
        }
        u16 qv[3][4];
#pragma unroll
        for (int t = 0; t < 3; ++t)
#pragma unroll
            for (int r = 0; r < 4; ++r) { const float f = acc[t][r]; qv[t][r] = f2bf(f); }
        if (s + 1 < NSEG) {
            const bool hi = c16 >= 8;
            v4u pk;
            pk.x = (unsigned)(hi ? qv[0][2] : qv[0][0]) | ((unsigned)(hi ? qv[1][2] : qv[1][0]) << 16);
            pk.y = (unsigned)(hi ? qv[2][2] : qv[2][0]) | (tag << 16);
            pk.z = (unsigned)(hi ? qv[0][3] : qv[0][1]) | ((unsigned)(hi ? qv[1][3] : qv[1][1]) << 16);
            pk.w = (unsigned)(hi ? qv[2][3] : qv[2][1]) | (tag << 16);
            st16(xr, (int)XA_OFF + (par * 64 + cu) * 4096 + tid * 16, pk);
        }
        if (c16 < 8) {
            const int token = c16;
            unsigned char* qk = lds + (j ? L_K : L_Q) + token * QK_STRIDE;
#pragma unroll
            for (int t = 0; t < 2; ++t) {
                v2u pk; pk.x = (unsigned)qv[t][0] | ((unsigned)qv[t][1] << 16); pk.y = (unsigned)qv[t][2] | ((unsigned)qv[t][3] << 16);
                *(v2u*)(qk + ((2 * w + t) * 16 + 4 * g) * 2) = pk;
            }
#pragma unroll
            for (int r = 0; r < 4; ++r) {
                const int e = j * 64 + w * 16 + 4 * g + r;
                *(u16*)(lds + L_VT + e * 32 + token * 2) = qv[2][r];
            }
        }
        auto store_kv = [&]() {
            if (c16 < 8) {
                if (j) {
#pragma unroll
                    for (int t = 0; t < 2; ++t) {
                        v2u pk; pk.x = (unsigned)qv[t][0] | ((unsigned)qv[t][1] << 16); pk.y = (unsigned)qv[t][2] | ((unsigned)qv[t][3] << 16);
                        st8(Km + (size_t)(s * MEMN + c16) * HD + h * DH + (2 * w + t) * 16 + 4 * g, pk);
                    }
                }
                v2u pk; pk.x = (unsigned)qv[2][0] | ((unsigned)qv[2][1] << 16); pk.y = (unsigned)qv[2][2] | ((unsigned)qv[2][3] << 16);
                st8(Vm + (size_t)(s * MEMN + c16) * HD + h * DH + j * 64 + w * 16 + 4 * g, pk);
            }
        };
        if (s + 1 == NSEG) {
            store_kv();
            asm volatile("s_waitcnt vmcnt(0)" ::: "memory");
            __syncthreads();
            if (tid == 0) __hip_atomic_fetch_add(flagp(a.ws, F_KM + s * 32 + h), 1u, RLX_AGENT);
        }
        if (s + 1 == NSEG) break;
        bf16x8 kx[2][4], vx[8];
#pragma unroll
        for (int f8 = 0; f8 < 8; ++f8) kx[f8 >> 2][f8 & 3] = *(const bf16x8*)(lds + L_KV + f8 * 1024 + lane * 16);
#pragma unroll
        for (int f8 = 0; f8 < 8; ++f8) vx[f8] = *(const bf16x8*)(lds + L_KV + (8 + f8) * 1024 + lane * 16);
        {
            v4u v[1];
            __builtin_amdgcn_s_sleep(SLEEP_A);
            const int offs[1] = {(int)XA_OFF + (par * 64 + (cu ^ 1)) * 4096 + tid * 16};
            gather<1, true>(xr, offs, tag, v, tmo);
            const int pj = j ^ 1;
            const int pw = tid >> 6, pg = (tid >> 4) & 3, token = tid & 7, r = ((tid >> 3) & 1) * 2;
            unsigned char* qk = lds + (pj ? L_K : L_Q) + token * QK_STRIDE;
            *(unsigned*)(qk + ((2 * pw) * 16 + 4 * pg + r) * 2) = (v[0].x & 0xffffu) | (v[0].z << 16);
            *(unsigned*)(qk + ((2 * pw + 1) * 16 + 4 * pg + r) * 2) = (v[0].x >> 16) | (v[0].z & 0xffff0000u);
            *(u16*)(lds + L_VT + (pj * 64 + pw * 16 + 4 * pg + r) * 32 + token * 2) = (u16)v[0].y;
            *(u16*)(lds + L_VT + (pj * 64 + pw * 16 + 4 * pg + r + 1) * 32 + token * 2) = (u16)v[0].w;
        }
        __syncthreads();
        store_kv();
        bf16x8 qf[4], kf[4];
        bf16x4 vm4[8];
#pragma unroll
        for (int ks = 0; ks < 4; ++ks) {
            qf[ks] = *(const bf16x8*)(lds + L_Q + (c16 & 7) * QK_STRIDE + ks * 64 + g * 16);
            kf[ks] = *(const bf16x8*)(lds + L_K + (c16 & 7) * QK_STRIDE + ks * 64 + g * 16);
        }
#pragma unroll
        for (int et = 0; et < 8; ++et) vm4[et] = *(const bf16x4*)(lds + L_VT + (et * 16 + c16) * 32 + g * 8);
        __builtin_amdgcn_sched_barrier(0);
        f32x4 st[3];
#pragma unroll
        for (int t = 0; t < 3; ++t) st[t] = (f32x4){0.f, 0.f, 0.f, 0.f};
#pragma unroll
        for (int ks = 0; ks < 4; ++ks) {
            st[1] = mfma32(kx[0][ks], qf[ks], st[1]);
            st[2] = mfma32(kx[1][ks], qf[ks], st[2]);
            st[0] = mfma32(kf[ks], qf[ks], st[0]);
        }
        __builtin_amdgcn_sched_barrier(0);
        bf16x8 wo[5][4];
#pragma unroll
        for (int nt = 0; nt < 5; ++nt)
#pragma unroll
            for (int ks = 0; ks < 4; ++ks) wo[nt][ks] = *(const bf16x8*)(lds + L_WO + ((w * 5 + nt) * 4 + ks) * 1024 + lane * 16);
        __builtin_amdgcn_sched_barrier(0);
        float x[3][4];
        float mx = -INFINITY;
#pragma unroll
        for (int t = 0; t < 3; ++t)
#pragma unroll
            for (int r = 0; r < 4; r += 2) {
                const float s0 = st[t][r], s1 = st[t][r + 1];
                const unsigned u = pk2bf(s0, s1);
                const unsigned u2 = pk2bf(__uint_as_float(u << 16) * scale, __uint_as_float(u & 0xffff0000u) * scale);
                float v0 = __uint_as_float(u2 << 16), v1 = __uint_as_float(u2 & 0xffff0000u);
                if (t == 0 && g >= 2) { v0 = -INFINITY; v1 = -INFINITY; }
                x[t][r] = v0; x[t][r + 1] = v1; mx = fmaxf(mx, fmaxf(v0, v1));
            }
        mx = xmax16_32(mx);
        const float mxl = -mx * 1.4426950408889634f;
        float sum = 0.f;
#pragma unroll
        for (int t = 0; t < 3; ++t)
#pragma unroll
            for (int r = 0; r < 4; ++r) { x[t][r] = __builtin_amdgcn_exp2f(__builtin_fmaf(x[t][r], 1.4426950408889634f, mxl)); sum += x[t][r]; }
        sum = xsum16_32(sum);
        const float inv = __builtin_amdgcn_rcpf(sum);
        v4u pxu, pmu;
        pmu.x = pk2bf(x[0][0] * inv, x[0][1] * inv); pmu.y = pk2bf(x[0][2] * inv, x[0][3] * inv); pmu.z = 0u; pmu.w = 0u;
        pxu.x = pk2bf(x[1][0] * inv, x[1][1] * inv); pxu.y = pk2bf(x[1][2] * inv, x[1][3] * inv);
        pxu.z = pk2bf(x[2][0] * inv, x[2][1] * inv); pxu.w = pk2bf(x[2][2] * inv, x[2][3] * inv);
        const bf16x8 px = __builtin_bit_cast(bf16x8, pxu), pm = __builtin_bit_cast(bf16x8, pmu);
        f32x4 oa[8];
#pragma unroll
        for (int et = 0; et < 8; ++et) oa[et] = mfma32(vx[et], px, (f32x4){0.f, 0.f, 0.f, 0.f});
#pragma unroll
        for (int et = 0; et < 8; ++et) {
            const bf16x8 vm = __builtin_shufflevector(vm4[et], (bf16x4){0, 0, 0, 0}, 0, 1, 2, 3, 4, 5, 6, 7);
            oa[et] = mfma32(vm, pm, oa[et]);
        }
        bf16x8 of[4];
#pragma unroll
        for (int et = 0; et < 8; ++et)
#pragma unroll
            for (int r = 0; r < 4; ++r) { const float f = oa[et][r]; of[et >> 1][(et & 1) * 4 + r] = (short)f2bf(f); }
        {
            f32x4 p[5];
#pragma unroll
            for (int nt = 0; nt < 5; ++nt) p[nt] = (f32x4){0.f, 0.f, 0.f, 0.f};
#pragma unroll
            for (int ks = 0; ks < 4; ++ks)
#pragma unroll
                for (int nt = 0; nt < 5; ++nt) p[nt] = mfma32(wo[nt][ks], of[ks], p[nt]);
            const int off = (int)XB_OFF + (par * 32 * 2560 + h * 2560 + (j * 4 + w) * 320 + lane) * 16;
            const bool hi = c16 >= 8;
#pragma unroll
            for (int nt = 0; nt < 5; ++nt) {
                const float p0 = p[nt][0], p1 = p[nt][1], p2 = p[nt][2], p3 = p[nt][3];
                v4u pk; pk.x = __float_as_uint(hi ? p2 : p0); pk.y = tag; pk.z = __float_as_uint(hi ? p3 : p1); pk.w = tag;
                st16(xr, off + nt * 1024, pk);
            }
        }
        {
            const int off = (int)XB_OFF + (par * 32 * 2560 + hg * 2560 + cu * 40 + pr) * 16;
            const int off5 = off + (hg < 2 ? 5 : 4) * 6 * 2560 * 16;
            v4u v[6];
            __builtin_amdgcn_s_sleep(SLEEP_B);
            constexpr int HS = 6 * 2560 * 16;
            const int offs[6] = {off, off + HS, off + 2 * HS, off + 3 * HS, off + 4 * HS, off5};
            gather<6, false>(xr, offs, tag, v, tmo);
            float s0 = 0.f, s1 = 0.f;
#pragma unroll
            for (int k = 0; k < 5; ++k) { s0 += __uint_as_float(v[k].x); s1 += __uint_as_float(v[k].z); }
            if (hg < 2) { s0 += __uint_as_float(v[5].x); s1 += __uint_as_float(v[5].z); }
            if (tid < 240) { v2u pk; pk.x = __float_as_uint(s0); pk.y = __float_as_uint(s1); *(v2u*)(lds + L_RED + tid * 8) = pk; }
        }
        asm volatile("s_waitcnt vmcnt(0)" ::: "memory");
        __syncthreads();
        if (tid == 192) __hip_atomic_fetch_add(flagp(a.ws, F_KM + s * 32 + h), 1u, RLX_AGENT);
        if (tid < 128) {
            const int t80 = tid < 80 ? tid : 79;
            const float* rp = (const float*)(lds + L_RED) + t80;
            const float sacc = ((rp[0] + rp[80]) + (rp[160] + rp[240])) + (rp[320] + rp[400]);
            const unsigned mine = f2bf(sacc);
            const unsigned m1 = (unsigned)__builtin_amdgcn_mov_dpp((int)mine, 0x55, 0xf, 0xf, true);
            const unsigned m2 = (unsigned)__builtin_amdgcn_mov_dpp((int)mine, 0xAA, 0xf, 0xf, true);
            const unsigned m3 = (unsigned)__builtin_amdgcn_mov_dpp((int)mine, 0xFF, 0xf, 0xf, true);
            if (tid < 80 && (tid & 3) == 0) {
                v4u pk; pk.x = mine | (m1 << 16); pk.y = tag; pk.z = m2 | (m3 << 16); pk.w = tag;
#pragma unroll
                for (int rp = 0; rp < XC_REP; ++rp) st16(xr, (int)XC_OFF + (par * XC_REP + rp) * 20480 + (cu * 20 + (tid >> 2)) * 16, pk);
            }
        }
    }
}

__device__ __forceinline__ void wait_flags(gu32* myflag, unsigned target, bool active, gu32* tmo) {
    if (threadIdx.x < 64) {
        for (unsigned spins = 0;; ++spins) {
            const bool ok = !active || (__hip_atomic_load(myflag, RLX_AGENT) >= target);
            if (__all(ok)) break;
            __builtin_amdgcn_s_sleep(8);
            if ((spins & 63u) == 63u) {
                if (spins > kSpinCap) { __hip_atomic_store(tmo, 1u, RLX_AGENT); break; }
                if (__hip_atomic_load(tmo, RLX_AGENT) != 0u) break;
            }
        }
        __builtin_amdgcn_fence(__ATOMIC_ACQUIRE, "agent");
        asm volatile("s_waitcnt vmcnt(0)" ::: "memory");
    }
    __syncthreads();
}
__device__ __forceinline__ void signal_flag(gu32* flag) {
    asm volatile("s_waitcnt vmcnt(0)" ::: "memory");
    __syncthreads();
    if (threadIdx.x == 0) __hip_atomic_fetch_add(flag, 1u, RLX_AGENT);
}

__device__ __forceinline__ void transpose_tile(const u16* __restrict__ S, int lds_, u16* D, int ldd, int k0, int n0, unsigned char* lds, unsigned char* ws) {
    const int tid = opaque_tid();
    u16* T = (u16*)lds;
#pragma unroll
    for (int i = 0; i < 2; ++i) {
        const int c = tid + 256 * i, row = c >> 3, ch = c & 7;
        const v4u v = *(const v4u*)(S + (size_t)(k0 + row) * lds_ + n0 + ch * 8);
        const unsigned wv[4] = {v.x, v.y, v.z, v.w};
#pragma unroll
        for (int e = 0; e < 4; ++e) {
            T[(ch * 8 + 2 * e) * 72 + row] = (u16)wv[e];
            T[(ch * 8 + 2 * e + 1) * 72 + row] = (u16)(wv[e] >> 16);
        }
    }
    __syncthreads();
    const rsrc_t dr = __builtin_amdgcn_make_buffer_rsrc((void*)D, 0, DM * ldd * 2, 0x00020000);
#pragma unroll
    for (int i = 0; i < 2; ++i) {
        const int c = tid + 256 * i, n = c >> 3, ch = c & 7;
        const v4u v = *(const v4u*)(T + n * 72 + ch * 8);
        st16(dr, ((n0 + n) * ldd + k0 + ch * 8) * 2, v);
    }
}

__device__ __forceinline__ void attn_x(const Args& a, int s, int h) {
    const int lane = opaque_tid() & 63, c16 = lane & 15, g = lane >> 4;
    const u16* Q = (const u16*)(a.ws + Q_OFF); const u16* K = (const u16*)(a.ws + K_OFF); const u16* Vt = (const u16*)(a.ws + VT_OFF);
    const u16* Km = (const u16*)(a.ws + KM_OFF); const u16* Vm = (const u16*)(a.ws + VM_OFF); u16* O = (u16*)(a.ws + O_OFF);
    const float scale = 0.08838834764831845f;
    bf16x8 qf[4][2], kf[4][3], vxf[8], vmf[8];
#pragma unroll
    for (int ks = 0; ks < 4; ++ks) {
        const int eo = h * DH + 32 * ks + 8 * g;
        qf[ks][0] = *(const bf16x8*)(Q + (size_t)(s * SEG + c16) * HD + eo);
        qf[ks][1] = *(const bf16x8*)(Q + (size_t)(s * SEG + 16 + c16) * HD + eo);
        kf[ks][1] = *(const bf16x8*)(K + (size_t)(s * SEG + c16) * HD + eo);
        kf[ks][2] = *(const bf16x8*)(K + (size_t)(s * SEG + 16 + c16) * HD + eo);
    }
#pragma unroll
    for (int et = 0; et < 8; ++et) vxf[et] = *(const bf16x8*)(Vt + ((size_t)s * HD + h * DH + et * 16 + c16) * 32 + 8 * g);
    {
        gu32* fl = flagp(a.ws, F_KM + s * 32 + h);
        gu32* tmo = (gu32*)(a.ws + TMO_OFF);
        for (unsigned spins = 0;; ++spins) {
            if (__builtin_amdgcn_readfirstlane(__hip_atomic_load(fl, RLX_AGENT)) >= 2u) break;
            __builtin_amdgcn_s_sleep(4);
            if ((spins & 63u) == 63u) {
                if (spins > 4 * kSpinCap) { __hip_atomic_store(tmo, 1u, RLX_AGENT); break; }
                if (__hip_atomic_load(tmo, RLX_AGENT) != 0u) break;
            }
        }
        __builtin_amdgcn_fence(__ATOMIC_ACQUIRE, "wavefront");
    }
    const rsrc_t kmr = __builtin_amdgcn_make_buffer_rsrc((void*)Km, 0, NSEG * MEMN * HD * 2, 0x00020000);
    const rsrc_t vmr = __builtin_amdgcn_make_buffer_rsrc((void*)Vm, 0, NSEG * MEMN * HD * 2, 0x00020000);
#pragma unroll
    for (int ks = 0; ks < 4; ++ks)
        kf[ks][0] = __builtin_bit_cast(bf16x8, __builtin_amdgcn_raw_buffer_load_b128(kmr, ((s * MEMN + (c16 & 7)) * HD + h * DH + 32 * ks + 8 * g) * 2, 0, 16));
#pragma unroll
    for (int et = 0; et < 8; ++et) {
#pragma unroll
        for (int r = 0; r < 4; ++r) {
            vmf[et][r] = (short)__builtin_amdgcn_raw_buffer_load_b16(vmr, ((s * MEMN + 4 * (g & 1) + r) * HD + h * DH + et * 16 + c16) * 2, 0, 16);
            vmf[et][4 + r] = 0;
        }
    }
    __builtin_amdgcn_sched_barrier(0);
    f32x4 st[3][2];
#pragma unroll
    for (int t = 0; t < 3; ++t) { st[t][0] = (f32x4){0.f, 0.f, 0.f, 0.f}; st[t][1] = (f32x4){0.f, 0.f, 0.f, 0.f}; }
#pragma unroll
    for (int ks = 0; ks < 4; ++ks)
#pragma unroll
        for (int t = 0; t < 3; ++t) { st[t][0] = mfma32(kf[ks][t], qf[ks][0], st[t][0]); st[t][1] = mfma32(kf[ks][t], qf[ks][1], st[t][1]); }
    bf16x8 px[2], pm[2];
#pragma unroll
    for (int qt = 0; qt < 2; ++qt) {
        const int qi = qt * 16 + c16;
        float x[3][4]; float mx = -INFINITY;
#pragma unroll
        for (int t = 0; t < 3; ++t)
#pragma unroll
            for (int r = 0; r < 4; ++r) {
                const float sv = st[t][qt][r];
                float v = rbf(rbf(sv) * scale);
                const int key = (t - 1) * 16 + 4 * g + r;
                const bool valid = (t == 0) ? (g < 2) : (key <= qi);
                if (!valid) v = -INFINITY;
                x[t][r] = v; mx = fmaxf(mx, v);
            }
        mx = xmax16_32(mx);
        float sum = 0.f;
#pragma unroll
        for (int t = 0; t < 3; ++t)
#pragma unroll
            for (int r = 0; r < 4; ++r) { x[t][r] = __expf(x[t][r] - mx); sum += x[t][r]; }
        sum = xsum16_32(sum);
        const float inv = 1.0f / sum;
#pragma unroll
        for (int r = 0; r < 4; ++r) {
            pm[qt][r] = (short)f2bf(x[0][r] * inv); pm[qt][4 + r] = 0;
            px[qt][r] = (short)f2bf(x[1][r] * inv);
            px[qt][4 + r] = (short)f2bf(x[2][r] * inv);
        }
    }
#pragma unroll
    for (int et = 0; et < 8; ++et) {
        const bf16x8 vx = vxf[et], vm = vmf[et];
#pragma unroll
        for (int qt = 0; qt < 2; ++qt) {
            f32x4 o = mfma32(vx, px[qt], (f32x4){0.f, 0.f, 0.f, 0.f});
            o = mfma32(vm, pm[qt], o);
            const float o0 = o[0], o1 = o[1], o2 = o[2], o3 = o[3];
            v2u pk;
            pk.x = (unsigned)f2bf(o0) | ((unsigned)f2bf(o1) << 16);
            pk.y = (unsigned)f2bf(o2) | ((unsigned)f2bf(o3) << 16);
            st8(O + (size_t)(s * SEG + qt * 16 + c16) * HD + h * DH + et * 16 + 4 * g, pk);
        }
    }
}

__device__ __forceinline__ void oproj_task(const Args& a, int s, int ns, unsigned char* lds) {
    const int tid = opaque_tid(), w = tid >> 6, lane = tid & 63, c16 = lane & 15, g = lane >> 4;
    const u16* O = (const u16*)(a.ws + O_OFF) + (size_t)(s * SEG + c16) * HD + w * 1024 + 8 * g;
    const u16* Wt = (const u16*)(a.ws + WOT_OFF) + (size_t)(ns * 64 + c16) * HD + w * 1024 + 8 * g;
    f32x4 acc[4][2];
#pragma unroll
    for (int nt = 0; nt < 4; ++nt) { acc[nt][0] = (f32x4){0.f, 0.f, 0.f, 0.f}; acc[nt][1] = (f32x4){0.f, 0.f, 0.f, 0.f}; }
#pragma unroll 1
    for (int kb = 0; kb < 4; ++kb) {
        bf16x8 of[8][2], wf[8][4];
#pragma unroll
        for (int k8 = 0; k8 < 8; ++k8) {
            const int ko = (kb * 8 + k8) * 32;
            of[k8][0] = *(const bf16x8*)(O + ko); of[k8][1] = *(const bf16x8*)(O + (size_t)16 * HD + ko);
#pragma unroll
            for (int nt = 0; nt < 4; ++nt) wf[k8][nt] = *(const bf16x8*)(Wt + (size_t)nt * 16 * HD + ko);
        }
        __builtin_amdgcn_sched_barrier(0);
#pragma unroll
        for (int k8 = 0; k8 < 8; ++k8)
#pragma unroll
            for (int nt = 0; nt < 4; ++nt) {
                acc[nt][0] = mfma32(wf[k8][nt], of[k8][0], acc[nt][0]);
                acc[nt][1] = mfma32(wf[k8][nt], of[k8][1], acc[nt][1]);
            }
    }
    f32x4* R = (f32x4*)lds;
#pragma unroll
    for (int nt = 0; nt < 4; ++nt) { R[(w * 8 + nt * 2) * 64 + lane] = acc[nt][0]; R[(w * 8 + nt * 2 + 1) * 64 + lane] = acc[nt][1]; }
    __syncthreads();
#pragma unroll
    for (int i = 0; i < 2; ++i) {
        const int tile = w * 2 + i, nt = tile >> 1, mt = tile & 1;
        const f32x4 v = (R[tile * 64 + lane] + R[(8 + tile) * 64 + lane]) + (R[(16 + tile) * 64 + lane] + R[(24 + tile) * 64 + lane]);
        const float v0 = v[0], v1 = v[1], v2 = v[2], v3 = v[3];
        v2u pk;
        pk.x = (unsigned)f2bf(v0) | ((unsigned)f2bf(v1) << 16);
        pk.y = (unsigned)f2bf(v2) | ((unsigned)f2bf(v3) << 16);
        *(v2u*)(a.out + (size_t)(s * SEG + mt * 16 + c16) * (2 * DM) + ns * 64 + nt * 16 + 4 * g) = pk;
    }
    __syncthreads();
}

__device__ __forceinline__ void oproj_fine_task(const Args& a, int s, int n16, unsigned char* lds, gu32* tmo) {
    const int tid = opaque_tid(), w = tid >> 6, lane = tid & 63, c16 = lane & 15, g = lane >> 4;
    const u16* O = (const u16*)(a.ws + O_OFF) + (size_t)(s * SEG + c16) * HD + w * 1024 + 8 * g;
    const u16* Wt = (const u16*)(a.ws + WOT_OFF) + (size_t)(n16 * 16 + c16) * HD + w * 1024 + 8 * g;
    wait_flags(flagp(a.ws, F_TR), 740u, tid == 0, tmo);
    bf16x8 wf[32];
#pragma unroll
    for (int ks = 0; ks < 32; ++ks) wf[ks] = *(const bf16x8*)(Wt + ks * 32);
    wait_flags(flagp(a.ws, F_ATT + s), 8u, tid == 0, tmo);
    f32x4 acc[2] = {(f32x4){0.f, 0.f, 0.f, 0.f}, (f32x4){0.f, 0.f, 0.f, 0.f}};
#pragma unroll
    for (int kb = 0; kb < 2; ++kb) {
        bf16x8 of[16][2];
#pragma unroll
        for (int k = 0; k < 16; ++k) { of[k][0] = *(const bf16x8*)(O + (kb * 16 + k) * 32); of[k][1] = *(const bf16x8*)(O + (size_t)16 * HD + (kb * 16 + k) * 32); }
        __builtin_amdgcn_sched_barrier(0);
#pragma unroll
        for (int k = 0; k < 16; ++k) { acc[0] = mfma32(wf[kb * 16 + k], of[k][0], acc[0]); acc[1] = mfma32(wf[kb * 16 + k], of[k][1], acc[1]); }
    }
    f32x4* R = (f32x4*)lds;
    R[(w * 2) * 64 + lane] = acc[0]; R[(w * 2 + 1) * 64 + lane] = acc[1];
    __syncthreads();
    if (w < 2) {
        const f32x4 v = (R[w * 64 + lane] + R[(2 + w) * 64 + lane]) + (R[(4 + w) * 64 + lane] + R[(6 + w) * 64 + lane]);
        const float v0 = v[0], v1 = v[1], v2 = v[2], v3 = v[3];
        v2u pk;
        pk.x = (unsigned)f2bf(v0) | ((unsigned)f2bf(v1) << 16);
        pk.y = (unsigned)f2bf(v2) | ((unsigned)f2bf(v3) << 16);
        *(v2u*)(a.out + (size_t)(s * SEG + w * 16 + c16) * (2 * DM) + n16 * 16 + 4 * g) = pk;
    }
    __syncthreads();
}

__device__ __forceinline__ void bfinal_task(const Args& a, int rb32, unsigned char* lds) {
    const int tid = opaque_tid(), w = tid >> 6, lane = tid & 63, c16 = lane & 15, g = lane >> 4;
    const u16* Bq = (const u16*)(a.ws + BQKV_OFF);
    constexpr int OST = 1296;
    const float scale = 0.22360679774997896f;
    for (int it = 0; it < 4; ++it) {
        const int idx = it * 256 + tid, h = idx & 31, row = idx >> 5, pos = rb32 * 32 + row;
        const u16* qp = Bq + (size_t)pos * BD3 + h * BDH;
        float q[BDH];
#pragma unroll
        for (int d = 0; d < BDH; d += 2) { const unsigned u = *(const unsigned*)(qp + d); q[d] = bf2f((u16)u); q[d + 1] = bf2f((u16)(u >> 16)); }
        float lg[8]; float mx = -INFINITY;
#pragma unroll
        for (int wi = 0; wi < 8; ++wi) {
            const int kp = pos - 2 * wi;
            float v = -INFINITY;
            if (kp >= 0) {
                const u16* kpn = Bq + (size_t)kp * BD3 + DM + h * BDH;
                float d0 = 0.f;
#pragma unroll
                for (int d = 0; d < BDH; d += 2) { const unsigned u = *(const unsigned*)(kpn + d); d0 += q[d] * bf2f((u16)u); d0 += q[d + 1] * bf2f((u16)(u >> 16)); }
                v = rbf(rbf(d0) * scale);
            }
            lg[wi] = v; mx = fmaxf(mx, v);
        }
        float sum = 0.f;
#pragma unroll
        for (int wi = 0; wi < 8; ++wi) { lg[wi] = __expf(lg[wi] - mx); sum += lg[wi]; }
        const float inv = 1.0f / sum;
        float o[BDH];
#pragma unroll
        for (int d = 0; d < BDH; ++d) o[d] = 0.f;
#pragma unroll
        for (int wi = 0; wi < 8; ++wi) {
            const int kp = pos - 2 * wi;
            if (kp >= 0) {
                const float p = rbf(lg[wi] * inv);
                const u16* vpn = Bq + (size_t)kp * BD3 + 2 * DM + h * BDH;
#pragma unroll
                for (int d = 0; d < BDH; d += 2) { const unsigned u = *(const unsigned*)(vpn + d); o[d] += p * bf2f((u16)u); o[d + 1] += p * bf2f((u16)(u >> 16)); }
            }
        }
#pragma unroll
        for (int d = 0; d < BDH; d += 2) *(unsigned*)(lds + row * OST + (h * BDH + d) * 2) = (unsigned)f2bf(o[d]) | ((unsigned)f2bf(o[d + 1]) << 16);
    }
    __syncthreads();
    const u16* Wt = (const u16*)(a.ws + BWOT_OFF) + (size_t)(w * 160 + c16) * DM + 8 * g;
#pragma unroll 1
    for (int nh = 0; nh < 2; ++nh) {
        f32x4 acc[5][2];
#pragma unroll
        for (int nt = 0; nt < 5; ++nt) { acc[nt][0] = (f32x4){0.f, 0.f, 0.f, 0.f}; acc[nt][1] = (f32x4){0.f, 0.f, 0.f, 0.f}; }
#pragma unroll 4
        for (int ks = 0; ks < 20; ++ks) {
            const bf16x8 o0 = *(const bf16x8*)(lds + c16 * OST + ks * 64 + g * 16), o1 = *(const bf16x8*)(lds + (16 + c16) * OST + ks * 64 + g * 16);
#pragma unroll
            for (int nt = 0; nt < 5; ++nt) {
                const bf16x8 wf = *(const bf16x8*)(Wt + (size_t)(nh * 5 + nt) * 16 * DM + ks * 32);
                acc[nt][0] = mfma32(wf, o0, acc[nt][0]);
                acc[nt][1] = mfma32(wf, o1, acc[nt][1]);
            }
        }
#pragma unroll
        for (int nt = 0; nt < 5; ++nt)
#pragma unroll
            for (int mt = 0; mt < 2; ++mt) {
                const float v0 = acc[nt][mt][0], v1 = acc[nt][mt][1], v2 = acc[nt][mt][2], v3 = acc[nt][mt][3];
                v2u pk;
                pk.x = (unsigned)f2bf(v0) | ((unsigned)f2bf(v1) << 16);
                pk.y = (unsigned)f2bf(v2) | ((unsigned)f2bf(v3) << 16);
                *(v2u*)(a.out + (size_t)(rb32 * 32 + mt * 16 + c16) * (2 * DM) + DM + w * 160 + (nh * 5 + nt) * 16 + 4 * g) = pk;
            }
    }
    __syncthreads();
}

constexpr int NWORK = 192, NCOARSE = 56;
constexpr int T_KV = 0, T_TR = T_KV + 1024, T_Q = T_TR + 740, T_BQ = T_Q + 512, T_BF = T_BQ + 240, T_SEG = T_BF + 64, T_LAST = T_SEG + NCOARSE * 18, T_END = T_LAST + (NSEG - NCOARSE) * 48;

__device__ __forceinline__ void worker_role(const Args& a, unsigned char* lds) {
    const int wid = blockIdx.x - 64, tid = threadIdx.x, w = tid >> 6;
    gu32* tmo = (gu32*)(a.ws + TMO_OFF);
    u16* Q = (u16*)(a.ws + Q_OFF); u16* K = (u16*)(a.ws + K_OFF); u16* Vt = (u16*)(a.ws + VT_OFF); u16* Bq = (u16*)(a.ws + BQKV_OFF);
    for (int T = wid; T < T_END; T += NWORK) {
        if (T < T_TR) {
            const int rb = T >> 6, r = T & 63, hc = r & 31;
            if (r < 32) gemm_tile<0>(a.Ax, DM, a.Wk, HD, K, HD, DM, rb * 128, hc * 128, lds);
            else gemm_tile<1>(a.Ax, DM, a.Wv, HD, Vt, HD, DM, rb * 128, hc * 128, lds);
            signal_flag(flagp(a.ws, F_KV + rb * 32 + hc));
        } else if (T < T_Q) {
            const int t = T - T_TR;
            if (t < 640) transpose_tile(a.Wo, DM, (u16*)(a.ws + WOT_OFF), HD, (t / 10) * 64, (t % 10) * 64, lds, a.ws);
            else transpose_tile(a.Bwo, DM, (u16*)(a.ws + BWOT_OFF), DM, ((t - 640) / 10) * 64, ((t - 640) % 10) * 64, lds, a.ws);
            signal_flag(flagp(a.ws, F_TR));
        } else if (T < T_BQ) {
            const int t = T - T_Q, rb = t >> 5, hc = t & 31;
            gemm_tile<0>(a.Ax, DM, a.Wq, HD, Q, HD, DM, rb * 128, hc * 128, lds);
            signal_flag(flagp(a.ws, F_Q + rb * 32 + hc));
        } else if (T < T_BF) {
            const int t = T - T_BQ, rb = t / 15, nt = t - rb * 15;
            gemm_tile<0>(a.Bx, DM, a.Bw, BD3, Bq, BD3, DM, rb * 128, nt * 128, lds);
            signal_flag(flagp(a.ws, F_BQ + rb));
        } else if (T < T_SEG) {
            const int rb32 = T - T_BF, rb = rb32 >> 2;
            const int fi = tid == 0 ? F_BQ + rb : (tid == 1 ? F_BQ + (rb > 0 ? rb - 1 : 0) : F_TR);
            wait_flags(flagp(a.ws, fi), tid < 2 ? 15u : 740u, tid < 3, tmo);
            bfinal_task(a, rb32, lds);
        } else {
            const int t = T - T_SEG, s = T < T_LAST ? t / 18 : NCOARSE + (T - T_LAST) / 48, r = T < T_LAST ? t - s * 18 : (T - T_LAST) % 48, rb = s >> 2;
            if (T >= T_LAST && r >= 8) {
                oproj_fine_task(a, s, r - 8, lds, tmo);
            } else if (r < 8) {
                const int hh = r * 4 + (tid & 3), kind = (tid >> 2) & 1;
                wait_flags(flagp(a.ws, kind == 0 ? F_KV + rb * 32 + hh : F_Q + rb * 32 + hh), kind == 1 ? 1u : 2u, tid < 8, tmo);
                attn_x(a, s, r * 4 + w);
                signal_flag(flagp(a.ws, F_ATT + s));
            } else {
                wait_flags(flagp(a.ws, tid == 0 ? F_ATT + s : F_TR), tid == 0 ? 8u : 740u, tid < 2, tmo);
                oproj_task(a, s, r - 8, lds);
            }
        }
    }
}

__global__ __launch_bounds__(256, 1) void fused_kernel(Args a) {
    extern __shared__ __attribute__((aligned(16))) unsigned char lds[];
    if (blockIdx.x < 64) chain_role(a, lds);
    else worker_role(a, lds);
}

extern "C" void kernel_launch(void* const* d_in, const int* in_sizes, int n_in,
                              void* d_out, int out_size, void* d_ws, size_t ws_size,
                              hipStream_t stream) {
    const u16* Ax = (const u16*)d_in[0];
    const u16* mem0 = (const u16*)d_in[1];
    const u16* Wq = (const u16*)d_in[2];
    const u16* Wk = (const u16*)d_in[3];
    const u16* Wv = (const u16*)d_in[4];
    const u16* Wo = (const u16*)d_in[5];
    const u16* Bx = (const u16*)d_in[7];
    const u16* Bw = (const u16*)d_in[8];
    const u16* Bwo = (const u16*)d_in[9];
    unsigned char* ws = (unsigned char*)d_ws;
    u16* out = (u16*)d_out;

    (void)hipMemsetAsync(ws, 0, XCHG_BYTES, stream);
    Args a{Ax, mem0, Wq, Wk, Wv, Wo, Bx, Bw, Bwo, out, ws};
    (void)hipFuncSetAttribute((const void*)fused_kernel, hipFuncAttributeMaxDynamicSharedMemorySize, CHAIN_LDS);
    fused_kernel<<<256, 256, CHAIN_LDS, stream>>>(a);
}
```
